```python
import torch

def _softmax(x, axis=-1):
    return torch.softmax(x, dim=axis)
def _gelu(x):
    return torch.nn.functional.gelu(x, approximate="tanh")
def _silu(x):
    return torch.nn.functional.silu(x)
def _sigmoid(x):
    return torch.sigmoid(x)
def _layernorm(x, axis=-1, eps=1e-5):
    m = x.mean(dim=axis, keepdim=True)
    v = x.var(dim=axis, keepdim=True, unbiased=False)
    return (x - m) / torch.sqrt(v + eps)
def _scatter_add(tgt, idx, src, axis=0):
    return tgt.clone().index_add_(axis, idx, src)
def _segment_sum(src, seg, n):
    out = torch.zeros((n,) + src.shape[1:], dtype=src.dtype)
    return out.index_add_(0, seg, src)
def _pad(x, pw):
    flat = tuple(v for pair in reversed(pw) for v in pair)
    return torch.nn.functional.pad(x, flat)

kernel_name = 'concat_heads__linear_attn_block__grouped_kv_mqa__prompt'

def setup_inputs():

    _w = (lambda a: (lambda t: t.to(torch.bfloat16) if t.is_floating_point() else t)(torch.as_tensor(a)))
    def _A_setup():
        B, S, D = 1, 2048, 1024
        return {
            "x": _w((lambda *s: torch.randn(*s, dtype=torch.bfloat16) * 0.05)(B, S, D)),
            "wqkv": _w((lambda *s: torch.randn(*s, dtype=torch.bfloat16) * 0.8)(D, 3 * D)),
            "wo": _w((lambda *s: torch.randn(*s, dtype=torch.bfloat16) * 0.05)(D, D)),
        }
    _A_in = _A_setup()

    def _B_setup():
        B, S, D, H = 1, 2048, 1024, 32
        Dh = D // H
        return {
            "x": _w((lambda *s: torch.randn(*s, dtype=torch.bfloat16) * 0.05)(B, S, D)),
            "wq": _w((lambda *s: torch.randn(*s, dtype=torch.bfloat16) * 0.8)(D, H * Dh)),
            "wk": _w((lambda *s: torch.randn(*s, dtype=torch.bfloat16) * 0.8)(D, Dh)),
            "wv": _w((lambda *s: torch.randn(*s, dtype=torch.bfloat16) * 0.05)(D, Dh)),
            "wo": _w((lambda *s: torch.randn(*s, dtype=torch.bfloat16) * 0.05)(H * Dh, D)),
        }
    _B_in = _B_setup()

    return {
        "A_x": _A_in["x"],
        "A_wqkv": _A_in["wqkv"],
        "A_wo": _A_in["wo"],
        "B_x": _B_in["x"],
        "B_wq": _B_in["wq"],
        "B_wk": _B_in["wk"],
        "B_wv": _B_in["wv"],
        "B_wo": _B_in["wo"],
    }

def reference(A_x, A_wqkv, A_wo, B_x, B_wq, B_wk, B_wv, B_wo):
    def _A_ref(x, wqkv, wo):
        B, S, D = x.shape
        H = 32; Dh = D // H
        qkv = (lambda x, *d: x.permute(*d))((x @ wqkv).reshape(B, S, 3, H, Dh), 2, 0, 3, 1, 4)
        q, k, v = qkv[0], qkv[1], qkv[2]
        phi = lambda t: torch.where(t > 0, t + 1, torch.exp(t)) + 1e-6
        pq, pk = phi(q), phi(k)
        kv = torch.einsum("bhsd,bhse->bhde", pk, v)
        z = torch.einsum("bhsd->bhd", pk)
        num = torch.einsum("bhqd,bhde->bhqe", pq, kv)
        den = torch.einsum("bhqd,bhd->bhq", pq, z)[..., None] + 1e-6
        o = num / den
        return (lambda x, *d: x.permute(*d))(o, 0, 2, 1, 3).reshape(B, S, D) @ wo

    def _B_ref(x, wq, wk, wv, wo):
        B, S, D = x.shape
        H = 32
        Dh = D // H
        q = (lambda x, *d: x.permute(*d))((x @ wq).reshape(B, S, H, Dh), 0, 2, 1, 3)
        k = x @ wk
        v = x @ wv
        scores = torch.einsum("bhqd,bkd->bhqk", q, k) * (Dh ** -0.5)
        p = _softmax(scores, axis=-1)
        o = torch.einsum("bhqk,bkd->bhqd", p, v)
        return (lambda x, *d: x.permute(*d))(o, 0, 2, 1, 3).reshape(B, S, H * Dh) @ wo

    a = _A_ref(A_x, A_wqkv, A_wo)
    b = _B_ref(B_x, B_wq, B_wk, B_wv, B_wo)
    return torch.concatenate([a, b], axis=-1)
```

```cpp
#include <hip/hip_runtime.h>
#include <hip/hip_bf16.h>
typedef unsigned short u16;
__device__ __forceinline__ float b2f(u16 v){ return __uint_as_float(((unsigned)v)<<16); }
__device__ __forceinline__ u16 f2b(float f){ unsigned u=__float_as_uint(f); u += 0x7FFF + ((u>>16)&1); return (u16)(u>>16); }
__device__ __forceinline__ float rb(float f){ return b2f(f2b(f)); }

__global__ void gemm(const u16* A, const u16* W, u16* C, int M, int N, int K, int ldw, int ldc){
  __shared__ float As[16][17], Ws[16][17];
  int tx=threadIdx.x, ty=threadIdx.y; int row=blockIdx.y*16+ty, col=blockIdx.x*16+tx;
  float acc=0;
  for(int k0=0;k0<K;k0+=16){
    As[ty][tx]=b2f(A[(long)row*K+k0+tx]);
    Ws[ty][tx]=b2f(W[(long)(k0+ty)*ldw+col]);
    __syncthreads();
    for(int k=0;k<16;++k) acc+=As[ty][k]*Ws[k][tx];
    __syncthreads();
  }
  C[(long)row*ldc+col]=f2b(acc);
}
__device__ __forceinline__ float phi(float t){ float r = t>0 ? rb(t+1.f) : rb(__expf(t)); return rb(r+1e-6f); }
__global__ void kvz(const u16* qkv, float* kv, float* z){
  int h=blockIdx.x; int d=threadIdx.y, e=threadIdx.x;
  float acc=0, zz=0;
  for(int s=0;s<2048;++s){
    float pk=phi(b2f(qkv[(long)s*3072+1024+h*32+d]));
    float v=b2f(qkv[(long)s*3072+2048+h*32+e]);
    acc+=pk*v; zz+=pk;
  }
  kv[(h*32+d)*32+e]=rb(acc);
  if(e==0) z[h*32+d]=rb(zz);
}
__global__ void lin_o(const u16* qkv, const float* kv, const float* z, u16* o){
  int s=blockIdx.x; int t=threadIdx.x; int h=t>>5, e=t&31;
  float num=0, den=0;
  for(int d=0;d<32;++d){ float pq=phi(b2f(qkv[(long)s*3072+h*32+d])); num+=pq*kv[(h*32+d)*32+e]; den+=pq*z[h*32+d]; }
  num=rb(num); den=rb(rb(den)+1e-6f);
  o[(long)s*1024+t]=f2b(num/den);
}
__global__ void attn(const u16* q, const u16* k, const u16* v, u16* o){
  int s=blockIdx.x, h=blockIdx.y, t=threadIdx.x;
  __shared__ float p[2048]; __shared__ float red[256]; __shared__ float qq[32];
  if(t<32) qq[t]=b2f(q[(long)s*1024+h*32+t]);
  __syncthreads();
  float m=-1e30f;
  for(int j=t;j<2048;j+=256){ float a=0; for(int d=0;d<32;++d) a+=qq[d]*b2f(k[j*32+d]); a=rb(rb(a)*rb(0.17677669529663687f)); p[j]=a; m=fmaxf(m,a);}
  red[t]=m; __syncthreads();
  for(int o2=128;o2>0;o2>>=1){ if(t<o2) red[t]=fmaxf(red[t],red[t+o2]); __syncthreads(); }
  m=red[0]; __syncthreads();
  float sum=0; for(int j=t;j<2048;j+=256){ float e=__expf(p[j]-m); p[j]=e; sum+=e; }
  red[t]=sum; __syncthreads();
  for(int o2=128;o2>0;o2>>=1){ if(t<o2) red[t]+=red[t+o2]; __syncthreads(); }
  sum=red[0]; __syncthreads();
  int d=t&31, g=t>>5; float acc=0;
  for(int j=g;j<2048;j+=8) acc+=rb(p[j]/sum)*b2f(v[j*32+d]);
  red[t]=acc; __syncthreads();
  if(t<32){ float a=0; for(int g2=0;g2<8;++g2) a+=red[g2*32+t]; o[(long)s*1024+h*32+t]=f2b(a); }
}
extern "C" void kernel_launch(void* const* d_in, const int* in_sizes, int n_in, void* d_out, int out_size, void* d_ws, size_t ws_size, hipStream_t stream){
  const u16* Ax=(const u16*)d_in[0]; const u16* Awqkv=(const u16*)d_in[1]; const u16* Awo=(const u16*)d_in[2];
  const u16* Bx=(const u16*)d_in[3]; const u16* Bwq=(const u16*)d_in[4]; const u16* Bwk=(const u16*)d_in[5]; const u16* Bwv=(const u16*)d_in[6]; const u16* Bwo=(const u16*)d_in[7];
  u16* out=(u16*)d_out; char* ws=(char*)d_ws;
  u16* qkv=(u16*)ws; u16* oA=(u16*)(ws+(16<<20)); u16* qB=(u16*)(ws+(24<<20)); u16* kB=(u16*)(ws+(32<<20)); u16* vB=(u16*)(ws+(33<<20)); u16* oB=(u16*)(ws+(34<<20));
  float* kv=(float*)(ws+(40<<20)); float* z=(float*)(ws+(41<<20));
  dim3 b(16,16);
  gemm<<<dim3(3072/16,2048/16),b,0,stream>>>(Ax,Awqkv,qkv,2048,3072,1024,3072,3072);
  gemm<<<dim3(1024/16,2048/16),b,0,stream>>>(Bx,Bwq,qB,2048,1024,1024,1024,1024);
  gemm<<<dim3(2,2048/16),b,0,stream>>>(Bx,Bwk,kB,2048,32,1024,32,32);
  gemm<<<dim3(2,2048/16),b,0,stream>>>(Bx,Bwv,vB,2048,32,1024,32,32);
  kvz<<<32,dim3(32,32),0,stream>>>(qkv,kv,z);
  lin_o<<<2048,1024,0,stream>>>(qkv,kv,z,oA);
  attn<<<dim3(2048,32),256,0,stream>>>(qB,kB,vB,oB);
  gemm<<<dim3(1024/16,2048/16),b,0,stream>>>(oA,Awo,out,2048,1024,1024,1024,2048);
  gemm<<<dim3(1024/16,2048/16),b,0,stream>>>(oB,Bwo,out+1024,2048,1024,1024,1024,2048);
}
```

```cpp
#include <hip/hip_runtime.h>
#include <hip/hip_bf16.h>
#include <stdint.h>

typedef unsigned short u16;
typedef short    bf16x8 __attribute__((ext_vector_type(8)));
typedef short    s16x4  __attribute__((ext_vector_type(4)));
typedef float    f32x16 __attribute__((ext_vector_type(16)));
typedef float    f32x4  __attribute__((ext_vector_type(4)));
typedef float    f32x2  __attribute__((ext_vector_type(2)));
typedef __bf16   bf2_t  __attribute__((ext_vector_type(2)));
typedef unsigned u32x4  __attribute__((ext_vector_type(4)));
typedef unsigned u32x2  __attribute__((ext_vector_type(2)));
#define LAS __attribute__((address_space(3)))
typedef LAS const char* lds_cptr;
typedef LAS char* lds_ptr;
#define DI __device__ __forceinline__
#define MFMA32(a, b, c) __builtin_amdgcn_mfma_f32_32x32x16_bf16(a, b, c, 0, 0, 0)
#define MFMA16(a, b, c) __builtin_amdgcn_mfma_f32_16x16x32_bf16(a, b, c, 0, 0, 0)
#define SBAR() __builtin_amdgcn_sched_barrier(0)
#define PIN(x) asm volatile("" : "+v"(x))

constexpr float LOG2E = 1.4426950408889634f;
constexpr float QSCALE = 0.17677669529663687f * LOG2E;

DI unsigned cvtpk(float lo, float hi) { f32x2 v = {lo, hi}; return __builtin_bit_cast(unsigned, __builtin_convertvector(v, bf2_t)); }
DI float bflo(unsigned u) { return __uint_as_float(u << 16); }
DI float bfhi(unsigned u) { return __uint_as_float(u & 0xffff0000u); }
DI u32x4 make_srd(const void* p, unsigned bytes) {
  unsigned long long a = (unsigned long long)p;
  u32x4 r; r[0] = __builtin_amdgcn_readfirstlane((unsigned)a); r[1] = __builtin_amdgcn_readfirstlane((unsigned)(a >> 32)); r[2] = bytes; r[3] = 0x00020000u; return r;
}
DI void dma16(unsigned voff, u32x4 srd, unsigned soff, unsigned ldsaddr) {
  asm volatile("s_mov_b32 m0, %3\n\ts_nop 0\n\tbuffer_load_dwordx4 %0, %1, %2 offen lds" :: "v"(voff), "s"(srd), "s"(soff), "s"(ldsaddr) : "memory");
}
DI bf16x8 ldsr128(lds_cptr p) { return *(const LAS bf16x8*)p; }
DI s16x4 ldstr(lds_cptr p) { return __builtin_bit_cast(s16x4, __builtin_amdgcn_ds_read_tr16_b64_v4i16((LAS s16x4*)p)); }
DI bf16x8 cat8(s16x4 a, s16x4 b) { return __builtin_shufflevector(a, b, 0, 1, 2, 3, 4, 5, 6, 7); }
template <int N> DI void wait_bar() { asm volatile("s_waitcnt vmcnt(%0) lgkmcnt(0)\n\ts_barrier" :: "i"(N) : "memory"); }
DI bf16x8 pack8(const f32x16& a, int s) {
  u32x4 p = {cvtpk(a[8 * s], a[8 * s + 1]), cvtpk(a[8 * s + 2], a[8 * s + 3]), cvtpk(a[8 * s + 4], a[8 * s + 5]), cvtpk(a[8 * s + 6], a[8 * s + 7])};
  return __builtin_bit_cast(bf16x8, p);
}
DI float phi1(float a) {
  float u = __builtin_fmaf(a, LOG2E, -LOG2E), e;
  asm("v_exp_f32_e64 %0, %1 clamp" : "=v"(e) : "v"(u));
  return __builtin_fmaxf(a, e) + 1e-6f;
}

constexpr size_t WS_QF = 0, WS_PQF = 4u << 20, WS_OAF = 8u << 20, WS_OBF = 12u << 20, WS_KVF = 16u << 20, WS_KMAX = WS_KVF + (256u << 10),
                 WS_ZP = WS_KMAX + 4096, WS_KVP = 17u << 20;

constexpr int NSLOT = 3;
template <bool XFRAG, bool SWAP, int EXTRA>
DI void gemm_loop(char* smem, const int w, const int lane, const u32x4 sx, const u32x4 sw, const u32x4 se, const unsigned (&vw)[4], const int ldw2  ,
                  const int xeb  , f32x16 (&acc)[2][2], f32x16& acce) {
  constexpr int STAGE = EXTRA ? 36864 : 32768;
  constexpr int ND = EXTRA ? 9 : 8;
  unsigned vx[4];
#pragma unroll
  for (int i = 0; i < 4; ++i) {
    if (XFRAG) vx[i] = w * 65536 + i * 1024 + lane * 16;
    else { const int row = 32 * w + 8 * i + (lane >> 3); vx[i] = row * 2048 + (((lane & 7) ^ ((row >> 1) & 7)) << 4); }
  }
  const unsigned ve = w * 1024 + lane * 16;
  const unsigned lds0 = (unsigned)(uintptr_t)smem;
  auto issue1 = [&](int s, int i) {
    const unsigned base = lds0 + (s % NSLOT) * STAGE;
    if (i < 4) dma16(vx[i], sx, s * (XFRAG ? 4096 : 128), base + w * 4096 + i * 1024);
    else if (i < 8) dma16(vw[i - 4], sw, s * 64 * ldw2, base + 16384 + w * 4096 + (i - 4) * 1024);
    else dma16(ve, se, s * 4096, base + 32768 + w * 1024);
  };
#pragma unroll
  for (int s = 0; s < NSLOT; ++s)
#pragma unroll
    for (int i = 0; i < ND; ++i) issue1(s, i);
  const int wm = w & 1, wn = w >> 1, r = lane & 31, h = lane >> 5;
  const int blk = (lane >> 4) & 1, q = (lane & 15) >> 2, p = lane & 3;
  lds_cptr xa[4], wa[2], xe, we;
#pragma unroll
  for (int ks = 0; ks < 4; ++ks) {
    if (XFRAG) xa[ks] = (lds_cptr)smem + wm * 8192 + ks * 1024 + lane * 16;
    else { const int row = 64 * wm + r; xa[ks] = (lds_cptr)smem + row * 128 + (((2 * ks + h) ^ ((row >> 1) & 7)) << 4); }
  }
#pragma unroll
  for (int fb = 0; fb < 2; ++fb)
    wa[fb] = (lds_cptr)smem + 16384 + ((XFRAG ? 4 : 8) * h + q) * 256 + (((4 * (2 * wn + fb) + 2 * blk + (p >> 1)) ^ (q << 2)) << 4) + 8 * (p & 1);
  constexpr int TOFF = XFRAG ? 2048 : 1024;
  { const int row = 32 * xeb + r; xe = (lds_cptr)smem + row * 128 + (((2 * w + h) ^ ((row >> 1) & 7)) << 4);
    we = (lds_cptr)smem + 32768 + (16 * w + 8 * h + q) * 64 + blk * 32 + p * 8; }
  bf16x8 xf[2][2], wf[2][2], xef, wef;
  auto rd = [&](int buf, int off, int ks) {
    xf[buf][0] = ldsr128(xa[ks] + off);
    wf[buf][0] = cat8(ldstr(wa[0] + off + ks * 4096), ldstr(wa[0] + off + ks * 4096 + TOFF));
    xf[buf][1] = ldsr128(xa[ks] + off + 4096);
    wf[buf][1] = cat8(ldstr(wa[1] + off + ks * 4096), ldstr(wa[1] + off + ks * 4096 + TOFF));
  };
  auto mm1 = [&](int buf, int tb, int fb) {
    if (SWAP) acc[tb][fb] = MFMA32(wf[buf][fb], xf[buf][tb], acc[tb][fb]);
    else acc[tb][fb] = MFMA32(xf[buf][tb], wf[buf][fb], acc[tb][fb]);
  };
  auto mmi = [&](int buf, int s, int i0) {
    mm1(buf, 0, 0); SBAR();
    if (s >= 0 && s < 16) issue1(s, i0);
    SBAR();
    mm1(buf, 0, 1); mm1(buf, 1, 0); SBAR();
    if (s >= 0 && s < 16) issue1(s, i0 + 1);
    SBAR();
    mm1(buf, 1, 1);
  };
  wait_bar<ND*(NSLOT - 1)>();
  rd(0, 0, 0);
#pragma unroll
  for (int s = 0; s < 16; ++s) {
    const int off = (s % NSLOT) * STAGE;
    const int sp = s >= 1 ? s - 1 + NSLOT : -1;
    rd(1, off, 1);
    if (EXTRA) { xef = ldsr128(xe + off); wef = cat8(ldstr(we + off), ldstr(we + off + 256)); }
    SBAR(); mmi(0, sp, 2); SBAR();
    rd(0, off, 2); SBAR(); mmi(1, sp, 4); SBAR();
    rd(1, off, 3); SBAR();
    if (EXTRA) { if (EXTRA == 1) acce = MFMA32(wef, xef, acce); else acce = MFMA32(xef, wef, acce); SBAR(); if (sp >= 0 && sp < 16) issue1(sp, 8); SBAR(); }
    mmi(0, sp, 6); SBAR();
    if (s < 15) {
      if (s + NSLOT - 1 <= 15) wait_bar<ND*(NSLOT - 2)>(); else wait_bar<0>();
      rd(0, ((s + 1) % NSLOT) * STAGE, 0); SBAR();
    }
    mmi(1, s + NSLOT, 0); SBAR();
  }
}

DI void w_offsets(unsigned (&vw)[4], int w, int lane, int ldw2, int seg2  , bool interleave) {
#pragma unroll
  for (int i = 0; i < 4; ++i) {
    const int k = 16 * w + 4 * i + (lane >> 4);
    const int c = (lane & 15) ^ ((k & 3) << 2);
    int colb;
    if (interleave) colb = ((c >> 2) & 1) * seg2 + (c >> 3) * 64 + (c & 3) * 16;
    else colb = c * 16;
    vw[i] = k * ldw2 + colb;
  }
}

template <int EXTRA>
DI void k1_body(char* smem, int w, int lane, const u16* x, const u16* wt, int ldw2, const u16* we, int rt, int ct, char* ws) {
  const u32x4 sx = make_srd(x + (size_t)rt * 128 * 1024, 128 * 2048);
  const u32x4 sw = make_srd(wt + ct * 128, 1024 * ldw2 - ct * 256);
  const u32x4 se = make_srd(we, 65536);
  unsigned vw[4]; w_offsets(vw, w, lane, ldw2, 0, false);
  f32x16 acc[2][2], acce = {};
  const float init = EXTRA ? 0.f : 1.f;
#pragma unroll
  for (int i = 0; i < 16; ++i) { acc[0][0][i] = init; acc[0][1][i] = init; acc[1][0][i] = init; acc[1][1][i] = init; }
  gemm_loop<false, true, EXTRA>(smem, w, lane, sx, sw, se, vw, ldw2, ct >> 1, acc, acce);
  const int wm = w & 1, wn = w >> 1;
  lds_ptr red = (lds_ptr)smem;
  if (EXTRA) {
    asm volatile("s_barrier" ::: "memory");
    if (w) {
#pragma unroll
      for (int j = 0; j < 4; ++j) *(LAS f32x4*)(red + w * 4096 + j * 1024 + lane * 16) = (f32x4){acce[4 * j], acce[4 * j + 1], acce[4 * j + 2], acce[4 * j + 3]};
    }
    asm volatile("s_waitcnt lgkmcnt(0)\n\ts_barrier" ::: "memory");
  }
  char* dst = ws + (EXTRA ? WS_QF : WS_PQF);
#pragma unroll
  for (int tb = 0; tb < 2; ++tb)
#pragma unroll
    for (int fb = 0; fb < 2; ++fb) {
      f32x16 a = acc[tb][fb];
#pragma unroll
      for (int i = 0; i < 16; ++i) a[i] = EXTRA ? a[i] * QSCALE : phi1(a[i]);
      char* d = dst + (size_t)((4 * rt + 2 * wm + tb) * 32 + 4 * ct + 2 * wn + fb) * 2048 + lane * 16;
      *(bf16x8*)d = pack8(a, 0); *(bf16x8*)(d + 1024) = pack8(a, 1);
    }
  if (EXTRA && w == 0) {
#pragma unroll
    for (int o = 1; o < 4; ++o)
#pragma unroll
      for (int j = 0; j < 4; ++j) { const f32x4 v = *(const LAS f32x4*)(red + o * 4096 + j * 1024 + lane * 16);
#pragma unroll
        for (int e = 0; e < 4; ++e) acce[4 * j + e] += v[e]; }
    const bf16x8 f0 = pack8(acce, 0), f1 = pack8(acce, 1);
    const int kt = 4 * rt + (ct >> 1);
    char* d = ws + WS_KVF + kt * 4096 + (EXTRA == 2 ? 2048 : 0) + lane * 16;
    *(bf16x8*)d = f0; *(bf16x8*)(d + 1024) = f1;
    const u32x4 u0 = __builtin_bit_cast(u32x4, f0), u1 = __builtin_bit_cast(u32x4, f1);
    float m = 0.f;
#pragma unroll
    for (int j = 0; j < 4; ++j) {
      const float a = bflo(u0[j]), b = bfhi(u0[j]), c = bflo(u1[j]), e = bfhi(u1[j]);
      if (EXTRA == 1) m += a * a + b * b + c * c + e * e;
      else m = fmaxf(fmaxf(m, fmaxf(fabsf(a), fabsf(b))), fmaxf(fabsf(c), fabsf(e)));
      if (EXTRA == 2 && !(a - a == 0.f && b - b == 0.f && c - c == 0.f && e - e == 0.f)) m = INFINITY;
    }
    if (EXTRA == 1) { m += __shfl_xor(m, 32); if (!(m == m)) m = INFINITY; }
#pragma unroll
    for (int o = 32; o; o >>= 1) m = fmaxf(m, __shfl_xor(m, o));
    if (lane == 0) ((float*)(ws + WS_KMAX))[(EXTRA == 2 ? 64 : 0) + kt] = m;
  }
}

__global__ __launch_bounds__(256, 2) void k1_proj(const u16* Ax, const u16* Awqkv, const u16* Bx, const u16* Bwq, const u16* Bwk, const u16* Bwv, char* ws) {
  extern __shared__ __attribute__((aligned(16))) char smem[];
  const int lane = threadIdx.x & 63, w = __builtin_amdgcn_readfirstlane(threadIdx.x >> 6);
  const int xcd = blockIdx.x & 7, idx = blockIdx.x >> 3;
  const int rt = (xcd & 3) * 4 + (idx & 3), ct = idx >> 2;
  if (xcd < 4) k1_body<0>(smem, w, lane, Ax, Awqkv, 6144, Bwk, rt, ct, ws);
  else if (ct & 1) k1_body<2>(smem, w, lane, Bx, Bwq, 2048, Bwv, rt, ct, ws);
  else k1_body<1>(smem, w, lane, Bx, Bwq, 2048, Bwk, rt, ct, ws);
}

__global__ __launch_bounds__(256, 2) void k2a_kv(const u16* Ax, const u16* Awqkv, char* ws) {
  extern __shared__ __attribute__((aligned(16))) char smem[];
  const int lane = threadIdx.x & 63, w = __builtin_amdgcn_readfirstlane(threadIdx.x >> 6);
  const int xcd = blockIdx.x & 7, idx = blockIdx.x >> 3;
  const int rt = (xcd & 1) * 8 + (idx & 7), hp = (xcd >> 1) * 4 + (idx >> 3);
  const u32x4 sx = make_srd(Ax + (size_t)rt * 128 * 1024, 128 * 2048);
  const u32x4 sw = make_srd(Awqkv + 1024 + hp * 64, 1024 * 6144 - (1024 + hp * 64) * 2);
  unsigned vw[4]; w_offsets(vw, w, lane, 6144, 2048, true);
  f32x16 acc[2][2], acce = {};
#pragma unroll
  for (int i = 0; i < 16; ++i) { acc[0][0][i] = 1.f; acc[0][1][i] = 0.f; acc[1][0][i] = 1.f; acc[1][1][i] = 0.f; }
  gemm_loop<false, false, 0>(smem, w, lane, sx, sw, sw, vw, 6144, 0, acc, acce);
  const int wm = w & 1, wn = w >> 1;
  f32x16 kv = {}, z = {};
  const bf16x8 ones = {0x3f80, 0x3f80, 0x3f80, 0x3f80, 0x3f80, 0x3f80, 0x3f80, 0x3f80};
#pragma unroll
  for (int tb = 0; tb < 2; ++tb) {
    f32x16 a = acc[tb][0];
#pragma unroll
    for (int i = 0; i < 16; ++i) a[i] = phi1(a[i]);
#pragma unroll
    for (int s = 0; s < 2; ++s) { const bf16x8 pk = pack8(a, s); kv = MFMA32(pk, pack8(acc[tb][1], s), kv); z = MFMA32(pk, ones, z); }
  }
  lds_ptr red = (lds_ptr)smem + wn * 8192;
  asm volatile("s_barrier" ::: "memory");
  if (wm) {
#pragma unroll
    for (int j = 0; j < 4; ++j) { *(LAS f32x4*)(red + j * 1024 + lane * 16) = (f32x4){kv[4 * j], kv[4 * j + 1], kv[4 * j + 2], kv[4 * j + 3]};
      *(LAS f32x4*)(red + 4096 + j * 1024 + lane * 16) = (f32x4){z[4 * j], z[4 * j + 1], z[4 * j + 2], z[4 * j + 3]}; }
  }
  asm volatile("s_waitcnt lgkmcnt(0)\n\ts_barrier" ::: "memory");
  if (!wm) {
    const int head = 2 * hp + wn;
    float* kp = (float*)(ws + WS_KVP) + (size_t)(head * 16 + rt) * 1024 + lane * 4;
    float* zp = (float*)(ws + WS_ZP) + (head * 16 + rt) * 32 + (lane >> 5) * 16;
#pragma unroll
    for (int j = 0; j < 4; ++j) {
      const f32x4 a = *(const LAS f32x4*)(red + j * 1024 + lane * 16), b = *(const LAS f32x4*)(red + 4096 + j * 1024 + lane * 16);
      *(f32x4*)(kp + j * 256) = a + (f32x4){kv[4 * j], kv[4 * j + 1], kv[4 * j + 2], kv[4 * j + 3]};
      if ((lane & 31) == 0) *(f32x4*)(zp + j * 4) = b + (f32x4){z[4 * j], z[4 * j + 1], z[4 * j + 2], z[4 * j + 3]};
    }
  }
}

DI void oa_wave(char* ws, int lane, int rb0, int head) {
  f32x16 kv = {}, z = {};
  const float* kp = (const float*)(ws + WS_KVP) + (size_t)head * 16 * 1024 + lane * 4;
  const float* zp = (const float*)(ws + WS_ZP) + head * 16 * 32 + (lane >> 5) * 16;
#pragma unroll 4
  for (int p = 0; p < 16; ++p)
#pragma unroll
    for (int j = 0; j < 4; ++j) {
      const f32x4 a = *(const f32x4*)(kp + p * 1024 + j * 256), b = *(const f32x4*)(zp + p * 32 + j * 4);
#pragma unroll
      for (int e = 0; e < 4; ++e) { kv[4 * j + e] += a[e]; z[4 * j + e] += b[e]; }
    }
  const bf16x8 kf0 = pack8(kv, 0), kf1 = pack8(kv, 1), zf0 = pack8(z, 0), zf1 = pack8(z, 1);
#pragma unroll
  for (int tb = 0; tb < 2; ++tb) {
    const size_t off = (size_t)((rb0 + tb) * 32 + head) * 2048 + lane * 16;
    const bf16x8 p0 = *(const bf16x8*)(ws + WS_PQF + off), p1 = *(const bf16x8*)(ws + WS_PQF + off + 1024);
    f32x16 num = {}, den = {};
    num = MFMA32(kf0, p0, num); den = MFMA32(zf0, p0, den);
    num = MFMA32(kf1, p1, num); den = MFMA32(zf1, p1, den);
    const float rcp = 1.f / (den[0] + 1e-6f);
#pragma unroll
    for (int i = 0; i < 16; ++i) num[i] *= rcp;
    *(bf16x8*)(ws + WS_OAF + off) = pack8(num, 0); *(bf16x8*)(ws + WS_OAF + off + 1024) = pack8(num, 1);
  }
}

constexpr int KVSLOT = 8192;
#define EX(x) __builtin_amdgcn_exp2f(x)
#define SLOT_(SN, K0, K1, Q0, Q1, SC, OP, LP, VP1, OC, LC, VC0, X1, X2) do { \
    float e0, e1, e2, e3, e4, e5, e6, e7, e8, e9; \
    SN = MFMA32(K0, Q0, zero16); SBAR(); \
    p1[3] = cvtpk(t14, t15); e0 = EX(SC[0]); e1 = EX(SC[1]); e2 = EX(SC[2]); p0[0] = cvtpk(e0, e1); PIN(p1); PIN(p0); PIN(e2); SBAR(); \
    SN = MFMA32(K1, Q1, SN); SBAR(); \
    X1; e3 = EX(SC[3]); e4 = EX(SC[4]); p0[1] = cvtpk(e2, e3); e5 = EX(SC[5]); PIN(p0); PIN(e4); PIN(e5); SBAR(); \
    LP = MFMA16(sel, __builtin_bit_cast(bf16x8, p1), LP); SBAR(); \
    e6 = EX(SC[6]); p0[2] = cvtpk(e4, e5); PIN(p0); PIN(e6); SBAR(); \
    OP = MFMA32(VP1, __builtin_bit_cast(bf16x8, p1), OP); SBAR(); \
    X2; e7 = EX(SC[7]); e8 = EX(SC[8]); p0[3] = cvtpk(e6, e7); e9 = EX(SC[9]); e0 = EX(SC[10]); PIN(p0); PIN(e8); PIN(e9); PIN(e0); SBAR(); \
    LC = MFMA16(sel, __builtin_bit_cast(bf16x8, p0), LC); SBAR(); \
    p1[0] = cvtpk(e8, e9); e1 = EX(SC[11]); PIN(p1); PIN(e1); SBAR(); \
    OC = MFMA32(VC0, __builtin_bit_cast(bf16x8, p0), OC); SBAR(); \
    e2 = EX(SC[12]); p1[1] = cvtpk(e0, e1); e3 = EX(SC[13]); t14 = EX(SC[14]); p1[2] = cvtpk(e2, e3); t15 = EX(SC[15]); PIN(p1); PIN(t14); PIN(t15); SBAR(); \
  } while (0)

DI void attn_wave(char* ring, char* ws, const int w, const int lane, const int qt0) {
  const u32x4 skv = make_srd(ws + WS_KVF, 262144);
  const unsigned lds0 = (unsigned)(uintptr_t)ring + w * 2048, vo = w * 2048 + lane * 16;
  auto issue = [&](int st) { const unsigned b = lds0 + (st & 3) * KVSLOT; dma16(vo, skv, st * KVSLOT, b); dma16(vo + 1024, skv, st * KVSLOT, b + 1024); };
  issue(0); issue(1); issue(2);
  const char* qp = ws + WS_QF + (size_t)qt0 * 2048 + lane * 16;
  const bf16x8 Qa0 = *(const bf16x8*)qp, Qa1 = *(const bf16x8*)(qp + 1024), Qb0 = *(const bf16x8*)(qp + 2048), Qb1 = *(const bf16x8*)(qp + 3072);
  float kmax = ((const float*)(ws + WS_KMAX))[lane], vmax = ((const float*)(ws + WS_KMAX))[64 + lane];
  float na = 0.f, nb = 0.f;
  { const u32x4 a0 = __builtin_bit_cast(u32x4, Qa0), a1 = __builtin_bit_cast(u32x4, Qa1), b0 = __builtin_bit_cast(u32x4, Qb0), b1 = __builtin_bit_cast(u32x4, Qb1);
#pragma unroll
    for (int j = 0; j < 4; ++j) {
      na += bflo(a0[j]) * bflo(a0[j]) + bfhi(a0[j]) * bfhi(a0[j]) + bflo(a1[j]) * bflo(a1[j]) + bfhi(a1[j]) * bfhi(a1[j]);
      nb += bflo(b0[j]) * bflo(b0[j]) + bfhi(b0[j]) * bfhi(b0[j]) + bflo(b1[j]) * bflo(b1[j]) + bfhi(b1[j]) * bfhi(b1[j]);
    } }
  na += __shfl_xor(na, 32); nb += __shfl_xor(nb, 32);
#pragma unroll
  for (int o = 32; o; o >>= 1) { kmax = fmaxf(kmax, __shfl_xor(kmax, o)); vmax = fmaxf(vmax, __shfl_xor(vmax, o)); }
  const bool ok = (na * kmax <= 3500.f) && (nb * kmax <= 3500.f) && (vmax >= 0x1p-40f) && (vmax <= 0x1p50f);
  const bool fast = __all(ok);
  const short one = (((lane >> 4) ^ (lane >> 2)) & 1) ? (short)0 : (short)0x3f80;
  const bf16x8 sel = {one, one, one, one, one, one, one, one};
  f32x16 Oa = {}, Ob = {}; f32x4 La = {}, Lb = {};
  const f32x16 zero16 = {};
  const lds_cptr rb = (lds_cptr)ring + lane * 16;
  wait_bar<4>();
  if (fast) {
    bf16x8 KA0 = ldsr128(rb), KA1 = ldsr128(rb + 1024), VA0 = ldsr128(rb + 2048), VA1 = ldsr128(rb + 3072), KB0, KB1, VB0, VB1 = {};
    f32x16 SX, SY; u32x4 p0 = {}, p1 = {}; float t14 = 0.f, t15 = 0.f;
    SX = MFMA32(KA0, Qa0, zero16); SX = MFMA32(KA1, Qa1, SX);
    for (int n = 0; n < 32; ++n) {
      const lds_cptr c = rb + (n & 3) * KVSLOT, nx = rb + ((n + 1) & 3) * KVSLOT;
      SLOT_(SY, KA0, KA1, Qb0, Qb1, SX, Ob, Lb, VB1, Oa, La, VA0, KB0 = ldsr128(c + 4096), KB1 = ldsr128(c + 5120));
      SLOT_(SX, KB0, KB1, Qa0, Qa1, SY, Oa, La, VA1, Ob, Lb, VA0, VB0 = ldsr128(c + 6144), VB1 = ldsr128(c + 7168));
      if (n < 30) wait_bar<2>(); else wait_bar<0>();
      if (n < 29) issue(n + 3);
      SLOT_(SY, KB0, KB1, Qb0, Qb1, SX, Ob, Lb, VA1, Oa, La, VB0, KA0 = ldsr128(nx), KA1 = ldsr128(nx + 1024));
      SLOT_(SX, KA0, KA1, Qa0, Qa1, SY, Oa, La, VB1, Ob, Lb, VB0, VA0 = ldsr128(nx + 2048), VA1 = ldsr128(nx + 3072));
    }
    p1[3] = cvtpk(t14, t15);
    Lb = MFMA16(sel, __builtin_bit_cast(bf16x8, p1), Lb); Ob = MFMA32(VB1, __builtin_bit_cast(bf16x8, p1), Ob);
  } else {
    float ma = -INFINITY, mb = -INFINITY;
    for (int n = 0; n < 32; ++n) {
      const lds_cptr c = rb + (n & 3) * KVSLOT;
#pragma unroll
      for (int kt = 0; kt < 2; ++kt) {
        const bf16x8 K0 = ldsr128(c + kt * 4096), K1 = ldsr128(c + kt * 4096 + 1024), V0 = ldsr128(c + kt * 4096 + 2048), V1 = ldsr128(c + kt * 4096 + 3072);
#pragma unroll
        for (int t = 0; t < 2; ++t) {
          f32x16 S = MFMA32(K0, t ? Qb0 : Qa0, zero16); S = MFMA32(K1, t ? Qb1 : Qa1, S);
          float mx = S[0];
#pragma unroll
          for (int i = 1; i < 16; ++i) mx = fmaxf(mx, S[i]);
          mx = fmaxf(mx, __shfl_xor(mx, 32));
          float& m = t ? mb : ma; f32x16& O = t ? Ob : Oa; f32x4& L = t ? Lb : La;
          const float mn = fmaxf(m, mx), al = EX(m - mn); m = mn;
#pragma unroll
          for (int i = 0; i < 16; ++i) { S[i] = EX(S[i] - mn); O[i] *= al; }
          L *= al;
          const bf16x8 P0 = pack8(S, 0), P1 = pack8(S, 1);
          O = MFMA32(V0, P0, O); O = MFMA32(V1, P1, O); L = MFMA16(sel, P0, L); L = MFMA16(sel, P1, L);
        }
      }
      if (n < 30) wait_bar<2>(); else wait_bar<0>();
      if (n < 29) issue(n + 3);
    }
  }
  const float ra = 1.f / La[0], rbb = 1.f / Lb[0];
#pragma unroll
  for (int i = 0; i < 16; ++i) { Oa[i] *= ra; Ob[i] *= rbb; }
  char* op = ws + WS_OBF + (size_t)qt0 * 2048 + lane * 16;
  *(bf16x8*)op = pack8(Oa, 0); *(bf16x8*)(op + 1024) = pack8(Oa, 1); *(bf16x8*)(op + 2048) = pack8(Ob, 0); *(bf16x8*)(op + 3072) = pack8(Ob, 1);
}

__global__ __launch_bounds__(256, 2) void k2b_attn(char* ws) {
  extern __shared__ __attribute__((aligned(16))) char smem[];
  const int lane = threadIdx.x & 63, w = __builtin_amdgcn_readfirstlane(threadIdx.x >> 6);
  const int b = blockIdx.x;
  oa_wave(ws, lane, (b >> 4) * 4 + (w & 1) * 2, (b & 15) * 2 + (w >> 1));
  attn_wave(smem, ws, w, lane, b * 8 + w * 2);
}

__global__ __launch_bounds__(256, 2) void k3_out(const u16* Awo, const u16* Bwo, char* ws, u16* out) {
  extern __shared__ __attribute__((aligned(16))) char smem[];
  const int lane = threadIdx.x & 63, w = __builtin_amdgcn_readfirstlane(threadIdx.x >> 6);
  const int xcd = blockIdx.x & 7, idx = blockIdx.x >> 3;
  const int br = xcd >> 2, rt = (xcd & 3) * 4 + (idx & 3), ct = idx >> 2;
  const u32x4 sx = make_srd(ws + (br ? WS_OBF : WS_OAF) + (size_t)rt * 262144, 262144);
  const u32x4 sw = make_srd((br ? Bwo : Awo) + ct * 128, 1024 * 2048 - ct * 256);
  unsigned vw[4]; w_offsets(vw, w, lane, 2048, 0, false);
  f32x16 acc[2][2] = {}, acce = {};
  gemm_loop<true, true, 0>(smem, w, lane, sx, sw, sw, vw, 2048, 0, acc, acce);
  const int wm = w & 1, wn = w >> 1, r = lane & 31, h = lane >> 5;
  asm volatile("s_barrier" ::: "memory");
  lds_ptr st = (lds_ptr)smem + w * 8192;
  u16* obase = out + (size_t)(rt * 128 + 64 * wm) * 2048 + br * 1024 + ct * 128 + 64 * wn;
#pragma unroll
  for (int t = 0; t < 2; ++t) {
#pragma unroll
    for (int fb = 0; fb < 2; ++fb) {
      const f32x16& a = acc[t][fb];
#pragma unroll
      for (int gp = 0; gp < 2; ++gp) {
        const unsigned a0 = cvtpk(a[8 * gp + 0], a[8 * gp + 1]), a1 = cvtpk(a[8 * gp + 2], a[8 * gp + 3]);
        const unsigned b0 = cvtpk(a[8 * gp + 4], a[8 * gp + 5]), b1 = cvtpk(a[8 * gp + 6], a[8 * gp + 7]);
        const auto s0 = __builtin_amdgcn_permlane32_swap(a0, b0, false, false);
        const auto s1 = __builtin_amdgcn_permlane32_swap(a1, b1, false, false);
        const u32x4 v = {s0[0], s1[0], s0[1], s1[1]};
        const int ch = 4 * fb + 2 * gp + h;
        *(LAS u32x4*)(st + (32 * t + r) * 128 + ((ch ^ (r & 7)) << 4)) = v;
      }
    }
#pragma unroll
    for (int i = 4 * t; i < 4 * t + 4; ++i) { const int row = 8 * i + (lane >> 3), ch = lane & 7;
      const u32x4 v = *(const LAS u32x4*)(st + row * 128 + ((ch ^ (row & 7)) << 4));
      *(u32x4*)(obase + (size_t)row * 2048 + ch * 8) = v; }
  }
}

extern "C" void kernel_launch(void* const* d_in, const int* in_sizes, int n_in, void* d_out, int out_size, void* d_ws, size_t ws_size, hipStream_t stream) {
  const u16* Ax = (const u16*)d_in[0]; const u16* Awqkv = (const u16*)d_in[1]; const u16* Awo = (const u16*)d_in[2];
  const u16* Bx = (const u16*)d_in[3]; const u16* Bwq = (const u16*)d_in[4]; const u16* Bwk = (const u16*)d_in[5]; const u16* Bwv = (const u16*)d_in[6]; const u16* Bwo = (const u16*)d_in[7];
  char* ws = (char*)d_ws;
  (void)hipFuncSetAttribute((const void*)k1_proj, hipFuncAttributeMaxDynamicSharedMemorySize, NSLOT * 36864);
  (void)hipFuncSetAttribute((const void*)k2a_kv, hipFuncAttributeMaxDynamicSharedMemorySize, NSLOT * 32768);
  (void)hipFuncSetAttribute((const void*)k3_out, hipFuncAttributeMaxDynamicSharedMemorySize, NSLOT * 32768);
  k1_proj<<<256, 256, NSLOT * 36864, stream>>>(Ax, Awqkv, Bx, Bwq, Bwk, Bwv, ws);
  k2a_kv<<<256, 256, NSLOT * 32768, stream>>>(Ax, Awqkv, ws);
  k2b_attn<<<256, 256, 4 * KVSLOT, stream>>>(ws);
  k3_out<<<256, 256, NSLOT * 32768, stream>>>(Awo, Bwo, ws, (u16*)d_out);
}
```

```cpp
#include <hip/hip_runtime.h>
#include <hip/hip_bf16.h>
#include <stdint.h>
#include <type_traits>

typedef unsigned short u16;
typedef short    bf16x8 __attribute__((ext_vector_type(8)));
typedef short    s16x4  __attribute__((ext_vector_type(4)));
typedef float    f32x16 __attribute__((ext_vector_type(16)));
typedef float    f32x4  __attribute__((ext_vector_type(4)));
typedef float    f32x2  __attribute__((ext_vector_type(2)));
typedef __bf16   bf2_t  __attribute__((ext_vector_type(2)));
typedef unsigned u32x4  __attribute__((ext_vector_type(4)));
typedef unsigned u32x2  __attribute__((ext_vector_type(2)));
#define LAS __attribute__((address_space(3)))
typedef LAS const char* lds_cptr;
typedef LAS char* lds_ptr;
#define DI __device__ __forceinline__
#define MFMA32(a, b, c) __builtin_amdgcn_mfma_f32_32x32x16_bf16(a, b, c, 0, 0, 0)
#define MFMA16(a, b, c) __builtin_amdgcn_mfma_f32_16x16x32_bf16(a, b, c, 0, 0, 0)
#define SBAR() __builtin_amdgcn_sched_barrier(0)
#define PIN(x) asm volatile("" : "+v"(x))

constexpr float LOG2E = 1.4426950408889634f;
constexpr float QSCALE = 0.17677669529663687f * LOG2E;

DI unsigned cvtpk(float lo, float hi) { f32x2 v = {lo, hi}; return __builtin_bit_cast(unsigned, __builtin_convertvector(v, bf2_t)); }
DI float bflo(unsigned u) { return __uint_as_float(u << 16); }
DI float bfhi(unsigned u) { return __uint_as_float(u & 0xffff0000u); }
DI u32x4 make_srd(const void* p, unsigned bytes) {
  unsigned long long a = (unsigned long long)p;
  u32x4 r; r[0] = __builtin_amdgcn_readfirstlane((unsigned)a); r[1] = __builtin_amdgcn_readfirstlane((unsigned)(a >> 32)); r[2] = bytes; r[3] = 0x00020000u; return r;
}
DI bf16x8 ldsr128(lds_cptr p) { return *(const LAS bf16x8*)p; }
DI s16x4 ldstr(lds_cptr p) { return __builtin_bit_cast(s16x4, __builtin_amdgcn_ds_read_tr16_b64_v4i16((LAS s16x4*)p)); }
DI bf16x8 cat8(s16x4 a, s16x4 b) { return __builtin_shufflevector(a, b, 0, 1, 2, 3, 4, 5, 6, 7); }
template <int N> DI void wait_bar() { asm volatile("s_waitcnt vmcnt(%0) lgkmcnt(0)\n\ts_barrier" :: "i"(N) : "memory"); }
DI bf16x8 pack8(const f32x16& a, int s) {
  u32x4 p = {cvtpk(a[8 * s], a[8 * s + 1]), cvtpk(a[8 * s + 2], a[8 * s + 3]), cvtpk(a[8 * s + 4], a[8 * s + 5]), cvtpk(a[8 * s + 6], a[8 * s + 7])};
  return __builtin_bit_cast(bf16x8, p);
}
DI float phi1(float a) {
  const float e = __builtin_amdgcn_fmed3f(__builtin_amdgcn_exp2f(__builtin_fmaf(a, LOG2E, -LOG2E)), 0.f, 1.f);
  return __builtin_amdgcn_fmed3f(a, e, 3.4028235e38f) + 1e-6f;
}
DI float fadd1(float a, float b) { float r; asm("v_add_f32_e32 %0, %1, %2" : "=v"(r) : "v"(a), "v"(b)); return r; }

#define ATTN_ASM \
  "ds_read_b128 v[104:107], %4 offset:0\n\t" \
  "ds_read_b128 v[108:111], %4 offset:1024\n\t" \
  "ds_read_b128 v[120:123], %4 offset:2048\n\t" \
  "ds_read_b128 v[124:127], %4 offset:3072\n\t" \
  "v_mov_b32 v136, 0\n\t" \
  "v_mov_b32 v137, 0\n\t" \
  "v_mov_b32 v138, 0\n\t" \
  "v_mov_b32 v139, 0\n\t" \
  "v_mov_b32 v140, 0\n\t" \
  "v_mov_b32 v141, 0\n\t" \
  "v_mov_b32 v142, 0\n\t" \
  "v_mov_b32 v143, 0\n\t" \
  "v_mov_b32 v132, 0\n\t" \
  "v_mov_b32 v133, 0\n\t" \
  "v_mov_b32 v134, 0\n\t" \
  "v_mov_b32 v135, 0\n\t" \
  "s_mov_b32 %6, 5\n\t" \
  "s_waitcnt lgkmcnt(0)\n\t" \
  "v_mfma_f32_32x32x16_bf16 v[64:79], v[104:107], %8, 0\n\t" \
  "v_mfma_f32_32x32x16_bf16 v[64:79], v[108:111], %9, v[64:79]\n\t" \
  "s_nop 15\n\t" \
  "LA_%=:\n\t" \
  "v_mfma_f32_32x32x16_bf16 v[80:95], v[104:107], %10, 0\n\t" \
  "ds_read_b128 v[112:115], %4 offset:4096\n\t" \
  "v_exp_f32_e32 v64, v64\n\t" \
  "ds_read_b128 v[116:119], %4 offset:5120\n\t" \
  "v_exp_f32_e32 v65, v65\n\t" \
  "v_exp_f32_e32 v66, v66\n\t" \
  "v_cvt_pk_bf16_f32 v96, v64, v65\n\t" \
  "v_mfma_f32_32x32x16_bf16 v[80:95], v[108:111], %11, v[80:95]\n\t" \
  "s_sub_u32 %6, %6, 1\n\t" \
  "v_exp_f32_e32 v67, v67\n\t" \
  "s_cmp_eq_u32 %6, 0\n\t" \
  "v_exp_f32_e32 v68, v68\n\t" \
  "v_cvt_pk_bf16_f32 v97, v66, v67\n\t" \
  "v_exp_f32_e32 v69, v69\n\t" \
  "v_smfmac_f32_16x16x64_bf16 %3, %12, v[136:143], %17\n\t" \
  "v_exp_f32_e32 v70, v70\n\t" \
  "v_cvt_pk_bf16_f32 v98, v68, v69\n\t" \
  "v_mfma_f32_32x32x16_bf16 %1, v[132:135], v[140:143], %1\n\t" \
  "s_cselect_b64 vcc, -1, 0\n\t" \
  "v_exp_f32_e32 v71, v71\n\t" \
  "v_exp_f32_e32 v72, v72\n\t" \
  "v_cvt_pk_bf16_f32 v99, v70, v71\n\t" \
  "v_exp_f32_e32 v73, v73\n\t" \
  "v_exp_f32_e32 v74, v74\n\t" \
  "v_mfma_f32_32x32x16_bf16 %0, v[120:123], v[96:99], %0\n\t" \
  "v_cvt_pk_bf16_f32 v100, v72, v73\n\t" \
  "v_exp_f32_e32 v75, v75\n\t" \
  "v_exp_f32_e32 v76, v76\n\t" \
  "v_cvt_pk_bf16_f32 v101, v74, v75\n\t" \
  "v_exp_f32_e32 v77, v77\n\t" \
  "v_exp_f32_e32 v78, v78\n\t" \
  "s_waitcnt lgkmcnt(0)\n\t" \
  "v_exp_f32_e32 v79, v79\n\t" \
  "v_cvt_pk_bf16_f32 v102, v76, v77\n\t" \
  "v_cvt_pk_bf16_f32 v103, v78, v79\n\t" \
  "v_mfma_f32_32x32x16_bf16 v[64:79], v[112:115], %8, 0\n\t" \
  "ds_read_b128 v[128:131], %4 offset:6144\n\t" \
  "v_exp_f32_e32 v80, v80\n\t" \
  "ds_read_b128 v[132:135], %4 offset:7168\n\t" \
  "v_exp_f32_e32 v81, v81\n\t" \
  "v_exp_f32_e32 v82, v82\n\t" \
  "v_cvt_pk_bf16_f32 v136, v80, v81\n\t" \
  "v_mfma_f32_32x32x16_bf16 v[64:79], v[116:119], %9, v[64:79]\n\t" \
  "v_exp_f32_e32 v83, v83\n\t" \
  "v_exp_f32_e32 v84, v84\n\t" \
  "v_cvt_pk_bf16_f32 v137, v82, v83\n\t" \
  "v_exp_f32_e32 v85, v85\n\t" \
  "v_smfmac_f32_16x16x64_bf16 %2, %12, v[96:103], %17\n\t" \
  "v_exp_f32_e32 v86, v86\n\t" \
  "v_cvt_pk_bf16_f32 v138, v84, v85\n\t" \
  "v_mfma_f32_32x32x16_bf16 %0, v[124:127], v[100:103], %0\n\t" \
  "v_exp_f32_e32 v87, v87\n\t" \
  "v_exp_f32_e32 v88, v88\n\t" \
  "v_cvt_pk_bf16_f32 v139, v86, v87\n\t" \
  "v_exp_f32_e32 v89, v89\n\t" \
  "v_exp_f32_e32 v90, v90\n\t" \
  "v_mfma_f32_32x32x16_bf16 %1, v[120:123], v[136:139], %1\n\t" \
  "v_cvt_pk_bf16_f32 v140, v88, v89\n\t" \
  "v_exp_f32_e32 v91, v91\n\t" \
  "v_exp_f32_e32 v92, v92\n\t" \
  "v_cvt_pk_bf16_f32 v141, v90, v91\n\t" \
  "v_exp_f32_e32 v93, v93\n\t" \
  "v_exp_f32_e32 v94, v94\n\t" \
  "s_waitcnt vmcnt(4) lgkmcnt(0)\n\t" \
  "v_exp_f32_e32 v95, v95\n\t" \
  "s_barrier\n\t" \
  "v_cvt_pk_bf16_f32 v142, v92, v93\n\t" \
  "v_cvt_pk_bf16_f32 v143, v94, v95\n\t" \
  "v_mfma_f32_32x32x16_bf16 v[80:95], v[112:115], %10, 0\n\t" \
  "ds_read_b128 v[104:107], %4 offset:8192\n\t" \
  "v_exp_f32_e32 v64, v64\n\t" \
  "ds_read_b128 v[108:111], %4 offset:9216\n\t" \
  "v_exp_f32_e32 v65, v65\n\t" \
  "v_exp_f32_e32 v66, v66\n\t" \
  "v_cvt_pk_bf16_f32 v96, v64, v65\n\t" \
  "v_mfma_f32_32x32x16_bf16 v[80:95], v[116:119], %11, v[80:95]\n\t" \
  "v_exp_f32_e32 v67, v67\n\t" \
  "v_exp_f32_e32 v68, v68\n\t" \
  "v_cvt_pk_bf16_f32 v97, v66, v67\n\t" \
  "v_exp_f32_e32 v69, v69\n\t" \
  "v_smfmac_f32_16x16x64_bf16 %3, %12, v[136:143], %17\n\t" \
  "v_exp_f32_e32 v70, v70\n\t" \
  "v_cvt_pk_bf16_f32 v98, v68, v69\n\t" \
  "v_mfma_f32_32x32x16_bf16 %1, v[124:127], v[140:143], %1\n\t" \
  "v_exp_f32_e32 v71, v71\n\t" \
  "v_exp_f32_e32 v72, v72\n\t" \
  "v_cvt_pk_bf16_f32 v99, v70, v71\n\t" \
  "v_exp_f32_e32 v73, v73\n\t" \
  "v_exp_f32_e32 v74, v74\n\t" \
  "v_mfma_f32_32x32x16_bf16 %0, v[128:131], v[96:99], %0\n\t" \
  "v_cvt_pk_bf16_f32 v100, v72, v73\n\t" \
  "v_exp_f32_e32 v75, v75\n\t" \
  "v_exp_f32_e32 v76, v76\n\t" \
  "v_cvt_pk_bf16_f32 v101, v74, v75\n\t" \
  "v_exp_f32_e32 v77, v77\n\t" \
  "v_exp_f32_e32 v78, v78\n\t" \
  "s_waitcnt lgkmcnt(0)\n\t" \
  "v_exp_f32_e32 v79, v79\n\t" \
  "v_cvt_pk_bf16_f32 v102, v76, v77\n\t" \
  "v_cvt_pk_bf16_f32 v103, v78, v79\n\t" \
  "v_mfma_f32_32x32x16_bf16 v[64:79], v[104:107], %8, 0\n\t" \
  "ds_read_b128 v[120:123], %4 offset:10240\n\t" \
  "v_exp_f32_e32 v80, v80\n\t" \
  "ds_read_b128 v[124:127], %4 offset:11264\n\t" \
  "v_exp_f32_e32 v81, v81\n\t" \
  "v_exp_f32_e32 v82, v82\n\t" \
  "v_cvt_pk_bf16_f32 v136, v80, v81\n\t" \
  "v_mfma_f32_32x32x16_bf16 v[64:79], v[108:111], %9, v[64:79]\n\t" \
  "v_exp_f32_e32 v83, v83\n\t" \
  "v_exp_f32_e32 v84, v84\n\t" \
  "v_cvt_pk_bf16_f32 v137, v82, v83\n\t" \
  "v_exp_f32_e32 v85, v85\n\t" \
  "v_smfmac_f32_16x16x64_bf16 %2, %12, v[96:103], %17\n\t" \
  "v_exp_f32_e32 v86, v86\n\t" \
  "v_cvt_pk_bf16_f32 v138, v84, v85\n\t" \
  "v_mfma_f32_32x32x16_bf16 %0, v[132:135], v[100:103], %0\n\t" \
  "v_exp_f32_e32 v87, v87\n\t" \
  "v_exp_f32_e32 v88, v88\n\t" \
  "v_cvt_pk_bf16_f32 v139, v86, v87\n\t" \
  "v_exp_f32_e32 v89, v89\n\t" \
  "v_exp_f32_e32 v90, v90\n\t" \
  "v_mfma_f32_32x32x16_bf16 %1, v[128:131], v[136:139], %1\n\t" \
  "v_cvt_pk_bf16_f32 v140, v88, v89\n\t" \
  "v_exp_f32_e32 v91, v91\n\t" \
  "v_exp_f32_e32 v92, v92\n\t" \
  "v_cvt_pk_bf16_f32 v141, v90, v91\n\t" \
  "v_exp_f32_e32 v93, v93\n\t" \
  "v_exp_f32_e32 v94, v94\n\t" \
  "s_waitcnt lgkmcnt(0)\n\t" \
  "v_exp_f32_e32 v95, v95\n\t" \
  "v_cvt_pk_bf16_f32 v142, v92, v93\n\t" \
  "v_cvt_pk_bf16_f32 v143, v94, v95\n\t" \
  "v_mfma_f32_32x32x16_bf16 v[80:95], v[104:107], %10, 0\n\t" \
  "ds_read_b128 v[112:115], %4 offset:12288\n\t" \
  "v_exp_f32_e32 v64, v64\n\t" \
  "ds_read_b128 v[116:119], %4 offset:13312\n\t" \
  "v_exp_f32_e32 v65, v65\n\t" \
  "v_exp_f32_e32 v66, v66\n\t" \
  "v_cvt_pk_bf16_f32 v96, v64, v65\n\t" \
  "v_mfma_f32_32x32x16_bf16 v[80:95], v[108:111], %11, v[80:95]\n\t" \
  "v_exp_f32_e32 v67, v67\n\t" \
  "v_exp_f32_e32 v68, v68\n\t" \
  "v_cvt_pk_bf16_f32 v97, v66, v67\n\t" \
  "v_exp_f32_e32 v69, v69\n\t" \
  "v_smfmac_f32_16x16x64_bf16 %3, %12, v[136:143], %17\n\t" \
  "v_exp_f32_e32 v70, v70\n\t" \
  "v_cvt_pk_bf16_f32 v98, v68, v69\n\t" \
  "v_mfma_f32_32x32x16_bf16 %1, v[132:135], v[140:143], %1\n\t" \
  "v_exp_f32_e32 v71, v71\n\t" \
  "v_exp_f32_e32 v72, v72\n\t" \
  "v_cvt_pk_bf16_f32 v99, v70, v71\n\t" \
  "v_exp_f32_e32 v73, v73\n\t" \
  "v_exp_f32_e32 v74, v74\n\t" \
  "v_mfma_f32_32x32x16_bf16 %0, v[120:123], v[96:99], %0\n\t" \
  "v_cvt_pk_bf16_f32 v100, v72, v73\n\t" \
  "v_exp_f32_e32 v75, v75\n\t" \
  "v_exp_f32_e32 v76, v76\n\t" \
  "v_cvt_pk_bf16_f32 v101, v74, v75\n\t" \
  "v_exp_f32_e32 v77, v77\n\t" \
  "v_exp_f32_e32 v78, v78\n\t" \
  "s_waitcnt lgkmcnt(0)\n\t" \
  "v_exp_f32_e32 v79, v79\n\t" \
  "v_cvt_pk_bf16_f32 v102, v76, v77\n\t" \
  "v_cvt_pk_bf16_f32 v103, v78, v79\n\t" \
  "v_mfma_f32_32x32x16_bf16 v[64:79], v[112:115], %8, 0\n\t" \
  "ds_read_b128 v[128:131], %4 offset:14336\n\t" \
  "v_exp_f32_e32 v80, v80\n\t" \
  "ds_read_b128 v[132:135], %4 offset:15360\n\t" \
  "v_exp_f32_e32 v81, v81\n\t" \
  "v_exp_f32_e32 v82, v82\n\t" \
  "v_cvt_pk_bf16_f32 v136, v80, v81\n\t" \
  "v_mfma_f32_32x32x16_bf16 v[64:79], v[116:119], %9, v[64:79]\n\t" \
  "v_exp_f32_e32 v83, v83\n\t" \
  "v_exp_f32_e32 v84, v84\n\t" \
  "v_cvt_pk_bf16_f32 v137, v82, v83\n\t" \
  "v_exp_f32_e32 v85, v85\n\t" \
  "v_smfmac_f32_16x16x64_bf16 %2, %12, v[96:103], %17\n\t" \
  "v_exp_f32_e32 v86, v86\n\t" \
  "v_cvt_pk_bf16_f32 v138, v84, v85\n\t" \
  "v_mfma_f32_32x32x16_bf16 %0, v[124:127], v[100:103], %0\n\t" \
  "v_exp_f32_e32 v87, v87\n\t" \
  "v_exp_f32_e32 v88, v88\n\t" \
  "v_cvt_pk_bf16_f32 v139, v86, v87\n\t" \
  "v_exp_f32_e32 v89, v89\n\t" \
  "v_exp_f32_e32 v90, v90\n\t" \
  "v_mfma_f32_32x32x16_bf16 %1, v[120:123], v[136:139], %1\n\t" \
  "v_cvt_pk_bf16_f32 v140, v88, v89\n\t" \
  "v_exp_f32_e32 v91, v91\n\t" \
  "v_exp_f32_e32 v92, v92\n\t" \
  "v_cvt_pk_bf16_f32 v141, v90, v91\n\t" \
  "v_exp_f32_e32 v93, v93\n\t" \
  "v_exp_f32_e32 v94, v94\n\t" \
  "s_waitcnt vmcnt(2) lgkmcnt(0)\n\t" \
  "v_exp_f32_e32 v95, v95\n\t" \
  "s_barrier\n\t" \
  "v_cvt_pk_bf16_f32 v142, v92, v93\n\t" \
  "v_cvt_pk_bf16_f32 v143, v94, v95\n\t" \
  "v_mfma_f32_32x32x16_bf16 v[80:95], v[112:115], %10, 0\n\t" \
  "ds_read_b128 v[104:107], %4 offset:16384\n\t" \
  "v_exp_f32_e32 v64, v64\n\t" \
  "ds_read_b128 v[108:111], %4 offset:17408\n\t" \
  "v_exp_f32_e32 v65, v65\n\t" \
  "v_exp_f32_e32 v66, v66\n\t" \
  "v_cvt_pk_bf16_f32 v96, v64, v65\n\t" \
  "v_mfma_f32_32x32x16_bf16 v[80:95], v[116:119], %11, v[80:95]\n\t" \
  "v_exp_f32_e32 v67, v67\n\t" \
  "v_exp_f32_e32 v68, v68\n\t" \
  "v_cvt_pk_bf16_f32 v97, v66, v67\n\t" \
  "v_exp_f32_e32 v69, v69\n\t" \
  "v_smfmac_f32_16x16x64_bf16 %3, %12, v[136:143], %17\n\t" \
  "v_exp_f32_e32 v70, v70\n\t" \
  "v_cvt_pk_bf16_f32 v98, v68, v69\n\t" \
  "v_mfma_f32_32x32x16_bf16 %1, v[124:127], v[140:143], %1\n\t" \
  "v_exp_f32_e32 v71, v71\n\t" \
  "v_exp_f32_e32 v72, v72\n\t" \
  "v_cvt_pk_bf16_f32 v99, v70, v71\n\t" \
  "v_exp_f32_e32 v73, v73\n\t" \
  "v_exp_f32_e32 v74, v74\n\t" \
  "v_mfma_f32_32x32x16_bf16 %0, v[128:131], v[96:99], %0\n\t" \
  "v_cvt_pk_bf16_f32 v100, v72, v73\n\t" \
  "v_exp_f32_e32 v75, v75\n\t" \
  "v_exp_f32_e32 v76, v76\n\t" \
  "v_cvt_pk_bf16_f32 v101, v74, v75\n\t" \
  "v_exp_f32_e32 v77, v77\n\t" \
  "v_exp_f32_e32 v78, v78\n\t" \
  "s_waitcnt lgkmcnt(0)\n\t" \
  "v_exp_f32_e32 v79, v79\n\t" \
  "v_cvt_pk_bf16_f32 v102, v76, v77\n\t" \
  "v_cvt_pk_bf16_f32 v103, v78, v79\n\t" \
  "v_mfma_f32_32x32x16_bf16 v[64:79], v[104:107], %8, 0\n\t" \
  "ds_read_b128 v[120:123], %4 offset:18432\n\t" \
  "v_exp_f32_e32 v80, v80\n\t" \
  "ds_read_b128 v[124:127], %4 offset:19456\n\t" \
  "v_exp_f32_e32 v81, v81\n\t" \
  "v_exp_f32_e32 v82, v82\n\t" \
  "v_cvt_pk_bf16_f32 v136, v80, v81\n\t" \
  "v_mfma_f32_32x32x16_bf16 v[64:79], v[108:111], %9, v[64:79]\n\t" \
  "v_exp_f32_e32 v83, v83\n\t" \
  "v_exp_f32_e32 v84, v84\n\t" \
  "v_cvt_pk_bf16_f32 v137, v82, v83\n\t" \
  "v_exp_f32_e32 v85, v85\n\t" \
  "v_smfmac_f32_16x16x64_bf16 %2, %12, v[96:103], %17\n\t" \
  "v_exp_f32_e32 v86, v86\n\t" \
  "v_cvt_pk_bf16_f32 v138, v84, v85\n\t" \
  "v_mfma_f32_32x32x16_bf16 %0, v[132:135], v[100:103], %0\n\t" \
  "v_exp_f32_e32 v87, v87\n\t" \
  "v_exp_f32_e32 v88, v88\n\t" \
  "v_cvt_pk_bf16_f32 v139, v86, v87\n\t" \
  "v_exp_f32_e32 v89, v89\n\t" \
  "v_exp_f32_e32 v90, v90\n\t" \
  "v_mfma_f32_32x32x16_bf16 %1, v[128:131], v[136:139], %1\n\t" \
  "v_cvt_pk_bf16_f32 v140, v88, v89\n\t" \
  "v_exp_f32_e32 v91, v91\n\t" \
  "v_exp_f32_e32 v92, v92\n\t" \
  "v_cvt_pk_bf16_f32 v141, v90, v91\n\t" \
  "v_exp_f32_e32 v93, v93\n\t" \
  "v_exp_f32_e32 v94, v94\n\t" \
  "s_waitcnt lgkmcnt(0)\n\t" \
  "v_exp_f32_e32 v95, v95\n\t" \
  "v_cndmask_b32 %5, %4, %13, vcc\n\t" \
  "v_cvt_pk_bf16_f32 v142, v92, v93\n\t" \
  "v_cvt_pk_bf16_f32 v143, v94, v95\n\t" \
  "v_mfma_f32_32x32x16_bf16 v[80:95], v[104:107], %10, 0\n\t" \
  "ds_read_b128 v[112:115], %4 offset:20480\n\t" \
  "v_exp_f32_e32 v64, v64\n\t" \
  "ds_read_b128 v[116:119], %4 offset:21504\n\t" \
  "v_exp_f32_e32 v65, v65\n\t" \
  "v_exp_f32_e32 v66, v66\n\t" \
  "v_cvt_pk_bf16_f32 v96, v64, v65\n\t" \
  "v_mfma_f32_32x32x16_bf16 v[80:95], v[108:111], %11, v[80:95]\n\t" \
  "v_exp_f32_e32 v67, v67\n\t" \
  "v_exp_f32_e32 v68, v68\n\t" \
  "v_cvt_pk_bf16_f32 v97, v66, v67\n\t" \
  "v_exp_f32_e32 v69, v69\n\t" \
  "v_smfmac_f32_16x16x64_bf16 %3, %12, v[136:143], %17\n\t" \
  "v_exp_f32_e32 v70, v70\n\t" \
  "v_cvt_pk_bf16_f32 v98, v68, v69\n\t" \
  "v_mfma_f32_32x32x16_bf16 %1, v[132:135], v[140:143], %1\n\t" \
  "v_exp_f32_e32 v71, v71\n\t" \
  "v_exp_f32_e32 v72, v72\n\t" \
  "v_cvt_pk_bf16_f32 v99, v70, v71\n\t" \
  "v_exp_f32_e32 v73, v73\n\t" \
  "v_exp_f32_e32 v74, v74\n\t" \
  "v_mfma_f32_32x32x16_bf16 %0, v[120:123], v[96:99], %0\n\t" \
  "v_cvt_pk_bf16_f32 v100, v72, v73\n\t" \
  "v_exp_f32_e32 v75, v75\n\t" \
  "v_exp_f32_e32 v76, v76\n\t" \
  "v_cvt_pk_bf16_f32 v101, v74, v75\n\t" \
  "v_exp_f32_e32 v77, v77\n\t" \
  "v_exp_f32_e32 v78, v78\n\t" \
  "s_waitcnt lgkmcnt(0)\n\t" \
  "v_exp_f32_e32 v79, v79\n\t" \
  "v_cvt_pk_bf16_f32 v102, v76, v77\n\t" \
  "v_cvt_pk_bf16_f32 v103, v78, v79\n\t" \
  "v_mfma_f32_32x32x16_bf16 v[64:79], v[112:115], %8, 0\n\t" \
  "ds_read_b128 v[128:131], %4 offset:22528\n\t" \
  "v_exp_f32_e32 v80, v80\n\t" \
  "ds_read_b128 v[132:135], %4 offset:23552\n\t" \
  "v_exp_f32_e32 v81, v81\n\t" \
  "v_exp_f32_e32 v82, v82\n\t" \
  "v_cvt_pk_bf16_f32 v136, v80, v81\n\t" \
  "v_mfma_f32_32x32x16_bf16 v[64:79], v[116:119], %9, v[64:79]\n\t" \
  "v_exp_f32_e32 v83, v83\n\t" \
  "v_exp_f32_e32 v84, v84\n\t" \
  "v_cvt_pk_bf16_f32 v137, v82, v83\n\t" \
  "v_exp_f32_e32 v85, v85\n\t" \
  "v_smfmac_f32_16x16x64_bf16 %2, %12, v[96:103], %17\n\t" \
  "v_exp_f32_e32 v86, v86\n\t" \
  "v_cvt_pk_bf16_f32 v138, v84, v85\n\t" \
  "v_mfma_f32_32x32x16_bf16 %0, v[124:127], v[100:103], %0\n\t" \
  "v_exp_f32_e32 v87, v87\n\t" \
  "v_exp_f32_e32 v88, v88\n\t" \
  "v_cvt_pk_bf16_f32 v139, v86, v87\n\t" \
  "v_exp_f32_e32 v89, v89\n\t" \
  "v_exp_f32_e32 v90, v90\n\t" \
  "v_mfma_f32_32x32x16_bf16 %1, v[120:123], v[136:139], %1\n\t" \
  "v_cvt_pk_bf16_f32 v140, v88, v89\n\t" \
  "v_exp_f32_e32 v91, v91\n\t" \
  "v_exp_f32_e32 v92, v92\n\t" \
  "v_cvt_pk_bf16_f32 v141, v90, v91\n\t" \
  "v_exp_f32_e32 v93, v93\n\t" \
  "v_exp_f32_e32 v94, v94\n\t" \
  "s_waitcnt vmcnt(0) lgkmcnt(0)\n\t" \
  "v_exp_f32_e32 v95, v95\n\t" \
  "s_barrier\n\t" \
  "v_cvt_pk_bf16_f32 v142, v92, v93\n\t" \
  "v_cvt_pk_bf16_f32 v143, v94, v95\n\t" \
  "v_mfma_f32_32x32x16_bf16 v[80:95], v[112:115], %10, 0\n\t" \
  "ds_read_b128 v[104:107], %4 offset:24576\n\t" \
  "v_exp_f32_e32 v64, v64\n\t" \
  "ds_read_b128 v[108:111], %4 offset:25600\n\t" \
  "v_exp_f32_e32 v65, v65\n\t" \
  "v_exp_f32_e32 v66, v66\n\t" \
  "v_cvt_pk_bf16_f32 v96, v64, v65\n\t" \
  "v_mfma_f32_32x32x16_bf16 v[80:95], v[116:119], %11, v[80:95]\n\t" \
  "v_exp_f32_e32 v67, v67\n\t" \
  "v_exp_f32_e32 v68, v68\n\t" \
  "v_cvt_pk_bf16_f32 v97, v66, v67\n\t" \
  "v_exp_f32_e32 v69, v69\n\t" \
  "v_smfmac_f32_16x16x64_bf16 %3, %12, v[136:143], %17\n\t" \
  "v_exp_f32_e32 v70, v70\n\t" \
  "v_cvt_pk_bf16_f32 v98, v68, v69\n\t" \
  "v_mfma_f32_32x32x16_bf16 %1, v[124:127], v[140:143], %1\n\t" \
  "v_exp_f32_e32 v71, v71\n\t" \
  "v_exp_f32_e32 v72, v72\n\t" \
  "v_cvt_pk_bf16_f32 v99, v70, v71\n\t" \
  "v_exp_f32_e32 v73, v73\n\t" \
  "v_exp_f32_e32 v74, v74\n\t" \
  "v_mfma_f32_32x32x16_bf16 %0, v[128:131], v[96:99], %0\n\t" \
  "v_cvt_pk_bf16_f32 v100, v72, v73\n\t" \
  "v_exp_f32_e32 v75, v75\n\t" \
  "v_exp_f32_e32 v76, v76\n\t" \
  "v_cvt_pk_bf16_f32 v101, v74, v75\n\t" \
  "v_exp_f32_e32 v77, v77\n\t" \
  "v_exp_f32_e32 v78, v78\n\t" \
  "s_waitcnt lgkmcnt(0)\n\t" \
  "v_exp_f32_e32 v79, v79\n\t" \
  "v_cvt_pk_bf16_f32 v102, v76, v77\n\t" \
  "v_cvt_pk_bf16_f32 v103, v78, v79\n\t" \
  "v_mfma_f32_32x32x16_bf16 v[64:79], v[104:107], %8, 0\n\t" \
  "ds_read_b128 v[120:123], %4 offset:26624\n\t" \
  "v_exp_f32_e32 v80, v80\n\t" \
  "ds_read_b128 v[124:127], %4 offset:27648\n\t" \
  "v_exp_f32_e32 v81, v81\n\t" \
  "v_exp_f32_e32 v82, v82\n\t" \
  "v_cvt_pk_bf16_f32 v136, v80, v81\n\t" \
  "v_mfma_f32_32x32x16_bf16 v[64:79], v[108:111], %9, v[64:79]\n\t" \
  "v_exp_f32_e32 v83, v83\n\t" \
  "v_exp_f32_e32 v84, v84\n\t" \
  "v_cvt_pk_bf16_f32 v137, v82, v83\n\t" \
  "v_exp_f32_e32 v85, v85\n\t" \
  "v_smfmac_f32_16x16x64_bf16 %2, %12, v[96:103], %17\n\t" \
  "v_exp_f32_e32 v86, v86\n\t" \
  "v_cvt_pk_bf16_f32 v138, v84, v85\n\t" \
  "v_mfma_f32_32x32x16_bf16 %0, v[132:135], v[100:103], %0\n\t" \
  "v_exp_f32_e32 v87, v87\n\t" \
  "v_exp_f32_e32 v88, v88\n\t" \
  "v_cvt_pk_bf16_f32 v139, v86, v87\n\t" \
  "v_exp_f32_e32 v89, v89\n\t" \
  "v_exp_f32_e32 v90, v90\n\t" \
  "v_mfma_f32_32x32x16_bf16 %1, v[128:131], v[136:139], %1\n\t" \
  "v_cvt_pk_bf16_f32 v140, v88, v89\n\t" \
  "v_exp_f32_e32 v91, v91\n\t" \
  "v_exp_f32_e32 v92, v92\n\t" \
  "v_cvt_pk_bf16_f32 v141, v90, v91\n\t" \
  "v_exp_f32_e32 v93, v93\n\t" \
  "v_exp_f32_e32 v94, v94\n\t" \
  "s_waitcnt lgkmcnt(0)\n\t" \
  "v_exp_f32_e32 v95, v95\n\t" \
  "v_cvt_pk_bf16_f32 v142, v92, v93\n\t" \
  "v_cvt_pk_bf16_f32 v143, v94, v95\n\t" \
  "v_mfma_f32_32x32x16_bf16 v[80:95], v[104:107], %10, 0\n\t" \
  "ds_read_b128 v[112:115], %4 offset:28672\n\t" \
  "v_exp_f32_e32 v64, v64\n\t" \
  "ds_read_b128 v[116:119], %4 offset:29696\n\t" \
  "v_exp_f32_e32 v65, v65\n\t" \
  "v_exp_f32_e32 v66, v66\n\t" \
  "v_cvt_pk_bf16_f32 v96, v64, v65\n\t" \
  "v_mfma_f32_32x32x16_bf16 v[80:95], v[108:111], %11, v[80:95]\n\t" \
  "v_exp_f32_e32 v67, v67\n\t" \
  "v_exp_f32_e32 v68, v68\n\t" \
  "v_cvt_pk_bf16_f32 v97, v66, v67\n\t" \
  "v_exp_f32_e32 v69, v69\n\t" \
  "v_smfmac_f32_16x16x64_bf16 %3, %12, v[136:143], %17\n\t" \
  "v_exp_f32_e32 v70, v70\n\t" \
  "v_cvt_pk_bf16_f32 v98, v68, v69\n\t" \
  "v_mfma_f32_32x32x16_bf16 %1, v[132:135], v[140:143], %1\n\t" \
  "v_exp_f32_e32 v71, v71\n\t" \
  "v_exp_f32_e32 v72, v72\n\t" \
  "v_cvt_pk_bf16_f32 v99, v70, v71\n\t" \
  "v_exp_f32_e32 v73, v73\n\t" \
  "v_exp_f32_e32 v74, v74\n\t" \
  "v_mfma_f32_32x32x16_bf16 %0, v[120:123], v[96:99], %0\n\t" \
  "v_cvt_pk_bf16_f32 v100, v72, v73\n\t" \
  "v_exp_f32_e32 v75, v75\n\t" \
  "v_exp_f32_e32 v76, v76\n\t" \
  "v_cvt_pk_bf16_f32 v101, v74, v75\n\t" \
  "v_exp_f32_e32 v77, v77\n\t" \
  "v_exp_f32_e32 v78, v78\n\t" \
  "s_waitcnt lgkmcnt(0)\n\t" \
  "v_exp_f32_e32 v79, v79\n\t" \
  "v_cvt_pk_bf16_f32 v102, v76, v77\n\t" \
  "v_cvt_pk_bf16_f32 v103, v78, v79\n\t" \
  "v_mfma_f32_32x32x16_bf16 v[64:79], v[112:115], %8, 0\n\t" \
  "ds_read_b128 v[128:131], %4 offset:30720\n\t" \
  "v_exp_f32_e32 v80, v80\n\t" \
  "ds_read_b128 v[132:135], %4 offset:31744\n\t" \
  "v_exp_f32_e32 v81, v81\n\t" \
  "v_exp_f32_e32 v82, v82\n\t" \
  "v_cvt_pk_bf16_f32 v136, v80, v81\n\t" \
  "v_mfma_f32_32x32x16_bf16 v[64:79], v[116:119], %9, v[64:79]\n\t" \
  "v_exp_f32_e32 v83, v83\n\t" \
  "v_exp_f32_e32 v84, v84\n\t" \
  "v_cvt_pk_bf16_f32 v137, v82, v83\n\t" \
  "v_exp_f32_e32 v85, v85\n\t" \
  "v_smfmac_f32_16x16x64_bf16 %2, %12, v[96:103], %17\n\t" \
  "v_exp_f32_e32 v86, v86\n\t" \
  "v_cvt_pk_bf16_f32 v138, v84, v85\n\t" \
  "v_mfma_f32_32x32x16_bf16 %0, v[124:127], v[100:103], %0\n\t" \
  "v_exp_f32_e32 v87, v87\n\t" \
  "v_exp_f32_e32 v88, v88\n\t" \
  "v_cvt_pk_bf16_f32 v139, v86, v87\n\t" \
  "v_exp_f32_e32 v89, v89\n\t" \
  "v_exp_f32_e32 v90, v90\n\t" \
  "v_mfma_f32_32x32x16_bf16 %1, v[120:123], v[136:139], %1\n\t" \
  "v_cvt_pk_bf16_f32 v140, v88, v89\n\t" \
  "v_exp_f32_e32 v91, v91\n\t" \
  "v_exp_f32_e32 v92, v92\n\t" \
  "v_cvt_pk_bf16_f32 v141, v90, v91\n\t" \
  "v_exp_f32_e32 v93, v93\n\t" \
  "v_exp_f32_e32 v94, v94\n\t" \
  "s_waitcnt lgkmcnt(0)\n\t" \
  "v_exp_f32_e32 v95, v95\n\t" \
  "s_barrier\n\t" \
  "v_cvt_pk_bf16_f32 v142, v92, v93\n\t" \
  "v_cvt_pk_bf16_f32 v143, v94, v95\n\t" \
  "v_mfma_f32_32x32x16_bf16 v[80:95], v[112:115], %10, 0\n\t" \
  "ds_read_b128 v[104:107], %5 offset:0\n\t" \
  "v_exp_f32_e32 v64, v64\n\t" \
  "ds_read_b128 v[108:111], %5 offset:1024\n\t" \
  "v_exp_f32_e32 v65, v65\n\t" \
  "v_exp_f32_e32 v66, v66\n\t" \
  "v_cvt_pk_bf16_f32 v96, v64, v65\n\t" \
  "v_mfma_f32_32x32x16_bf16 v[80:95], v[116:119], %11, v[80:95]\n\t" \
  "v_exp_f32_e32 v67, v67\n\t" \
  "v_exp_f32_e32 v68, v68\n\t" \
  "v_cvt_pk_bf16_f32 v97, v66, v67\n\t" \
  "v_exp_f32_e32 v69, v69\n\t" \
  "v_smfmac_f32_16x16x64_bf16 %3, %12, v[136:143], %17\n\t" \
  "v_exp_f32_e32 v70, v70\n\t" \
  "v_cvt_pk_bf16_f32 v98, v68, v69\n\t" \
  "v_mfma_f32_32x32x16_bf16 %1, v[124:127], v[140:143], %1\n\t" \
  "v_exp_f32_e32 v71, v71\n\t" \
  "v_exp_f32_e32 v72, v72\n\t" \
  "v_cvt_pk_bf16_f32 v99, v70, v71\n\t" \
  "v_exp_f32_e32 v73, v73\n\t" \
  "v_exp_f32_e32 v74, v74\n\t" \
  "v_mfma_f32_32x32x16_bf16 %0, v[128:131], v[96:99], %0\n\t" \
  "v_cvt_pk_bf16_f32 v100, v72, v73\n\t" \
  "v_exp_f32_e32 v75, v75\n\t" \
  "v_exp_f32_e32 v76, v76\n\t" \
  "v_cvt_pk_bf16_f32 v101, v74, v75\n\t" \
  "v_exp_f32_e32 v77, v77\n\t" \
  "v_exp_f32_e32 v78, v78\n\t" \
  "s_waitcnt lgkmcnt(0)\n\t" \
  "v_exp_f32_e32 v79, v79\n\t" \
  "v_cvt_pk_bf16_f32 v102, v76, v77\n\t" \
  "v_cvt_pk_bf16_f32 v103, v78, v79\n\t" \
  "v_mfma_f32_32x32x16_bf16 v[64:79], v[104:107], %8, 0\n\t" \
  "ds_read_b128 v[120:123], %5 offset:2048\n\t" \
  "v_exp_f32_e32 v80, v80\n\t" \
  "ds_read_b128 v[124:127], %5 offset:3072\n\t" \
  "v_exp_f32_e32 v81, v81\n\t" \
  "v_exp_f32_e32 v82, v82\n\t" \
  "v_cvt_pk_bf16_f32 v136, v80, v81\n\t" \
  "v_mfma_f32_32x32x16_bf16 v[64:79], v[108:111], %9, v[64:79]\n\t" \
  "v_exp_f32_e32 v83, v83\n\t" \
  "v_exp_f32_e32 v84, v84\n\t" \
  "v_cvt_pk_bf16_f32 v137, v82, v83\n\t" \
  "v_exp_f32_e32 v85, v85\n\t" \
  "v_smfmac_f32_16x16x64_bf16 %2, %12, v[96:103], %17\n\t" \
  "v_exp_f32_e32 v86, v86\n\t" \
  "v_cvt_pk_bf16_f32 v138, v84, v85\n\t" \
  "v_mfma_f32_32x32x16_bf16 %0, v[132:135], v[100:103], %0\n\t" \
  "v_exp_f32_e32 v87, v87\n\t" \
  "v_exp_f32_e32 v88, v88\n\t" \
  "v_cvt_pk_bf16_f32 v139, v86, v87\n\t" \
  "v_exp_f32_e32 v89, v89\n\t" \
  "v_exp_f32_e32 v90, v90\n\t" \
  "v_mfma_f32_32x32x16_bf16 %1, v[128:131], v[136:139], %1\n\t" \
  "v_cvt_pk_bf16_f32 v140, v88, v89\n\t" \
  "v_exp_f32_e32 v91, v91\n\t" \
  "v_exp_f32_e32 v92, v92\n\t" \
  "v_cvt_pk_bf16_f32 v141, v90, v91\n\t" \
  "v_exp_f32_e32 v93, v93\n\t" \
  "v_exp_f32_e32 v94, v94\n\t" \
  "s_waitcnt lgkmcnt(0)\n\t" \
  "v_exp_f32_e32 v95, v95\n\t" \
  "v_cvt_pk_bf16_f32 v142, v92, v93\n\t" \
  "v_cvt_pk_bf16_f32 v143, v94, v95\n\t" \
  "s_cbranch_scc0 LA_%=\n\t" \
  "v_mov_b32 %4, %13\n\t" \
  "v_mov_b32 %5, %14\n\t" \
  "s_mov_b32 %6, 3\n\t" \
  "LB_%=:\n\t" \
  "v_mfma_f32_32x32x16_bf16 v[80:95], v[104:107], %10, 0\n\t" \
  "ds_read_b128 v[112:115], %4 offset:4096\n\t" \
  "v_exp_f32_e32 v64, v64\n\t" \
  "ds_read_b128 v[116:119], %4 offset:5120\n\t" \
  "v_exp_f32_e32 v65, v65\n\t" \
  "v_exp_f32_e32 v66, v66\n\t" \
  "v_cvt_pk_bf16_f32 v96, v64, v65\n\t" \
  "v_mfma_f32_32x32x16_bf16 v[80:95], v[108:111], %11, v[80:95]\n\t" \
  "s_sub_u32 %6, %6, 1\n\t" \
  "v_exp_f32_e32 v67, v67\n\t" \
  "s_cmp_eq_u32 %6, 0\n\t" \
  "v_exp_f32_e32 v68, v68\n\t" \
  "v_cvt_pk_bf16_f32 v97, v66, v67\n\t" \
  "v_exp_f32_e32 v69, v69\n\t" \
  "v_smfmac_f32_16x16x64_bf16 %3, %12, v[136:143], %17\n\t" \
  "v_exp_f32_e32 v70, v70\n\t" \
  "v_cvt_pk_bf16_f32 v98, v68, v69\n\t" \
  "v_mfma_f32_32x32x16_bf16 %1, v[132:135], v[140:143], %1\n\t" \
  "v_exp_f32_e32 v71, v71\n\t" \
  "v_exp_f32_e32 v72, v72\n\t" \
  "v_cvt_pk_bf16_f32 v99, v70, v71\n\t" \
  "v_exp_f32_e32 v73, v73\n\t" \
  "v_exp_f32_e32 v74, v74\n\t" \
  "v_mfma_f32_32x32x16_bf16 %0, v[120:123], v[96:99], %0\n\t" \
  "v_cvt_pk_bf16_f32 v100, v72, v73\n\t" \
  "v_exp_f32_e32 v75, v75\n\t" \
  "v_exp_f32_e32 v76, v76\n\t" \
  "v_cvt_pk_bf16_f32 v101, v74, v75\n\t" \
  "v_exp_f32_e32 v77, v77\n\t" \
  "v_exp_f32_e32 v78, v78\n\t" \
  "s_waitcnt lgkmcnt(0)\n\t" \
  "v_exp_f32_e32 v79, v79\n\t" \
  "v_cvt_pk_bf16_f32 v102, v76, v77\n\t" \
  "v_cvt_pk_bf16_f32 v103, v78, v79\n\t" \
  "v_mfma_f32_32x32x16_bf16 v[64:79], v[112:115], %8, 0\n\t" \
  "ds_read_b128 v[128:131], %4 offset:6144\n\t" \
  "v_exp_f32_e32 v80, v80\n\t" \
  "ds_read_b128 v[132:135], %4 offset:7168\n\t" \
  "v_exp_f32_e32 v81, v81\n\t" \
  "v_exp_f32_e32 v82, v82\n\t" \
  "v_cvt_pk_bf16_f32 v136, v80, v81\n\t" \
  "v_mfma_f32_32x32x16_bf16 v[64:79], v[116:119], %9, v[64:79]\n\t" \
  "v_exp_f32_e32 v83, v83\n\t" \
  "v_exp_f32_e32 v84, v84\n\t" \
  "v_cvt_pk_bf16_f32 v137, v82, v83\n\t" \
  "v_exp_f32_e32 v85, v85\n\t" \
  "v_smfmac_f32_16x16x64_bf16 %2, %12, v[96:103], %17\n\t" \
  "v_exp_f32_e32 v86, v86\n\t" \
  "v_cvt_pk_bf16_f32 v138, v84, v85\n\t" \
  "v_mfma_f32_32x32x16_bf16 %0, v[124:127], v[100:103], %0\n\t" \
  "v_exp_f32_e32 v87, v87\n\t" \
  "v_exp_f32_e32 v88, v88\n\t" \
  "v_cvt_pk_bf16_f32 v139, v86, v87\n\t" \
  "v_exp_f32_e32 v89, v89\n\t" \
  "v_exp_f32_e32 v90, v90\n\t" \
  "v_mfma_f32_32x32x16_bf16 %1, v[120:123], v[136:139], %1\n\t" \
  "v_cvt_pk_bf16_f32 v140, v88, v89\n\t" \
  "v_exp_f32_e32 v91, v91\n\t" \
  "v_exp_f32_e32 v92, v92\n\t" \
  "v_cvt_pk_bf16_f32 v141, v90, v91\n\t" \
  "v_exp_f32_e32 v93, v93\n\t" \
  "v_exp_f32_e32 v94, v94\n\t" \
  "s_waitcnt lgkmcnt(0)\n\t" \
  "v_exp_f32_e32 v95, v95\n\t" \
  "v_cvt_pk_bf16_f32 v142, v92, v93\n\t" \
  "v_cvt_pk_bf16_f32 v143, v94, v95\n\t" \
  "v_mfma_f32_32x32x16_bf16 v[80:95], v[112:115], %10, 0\n\t" \
  "ds_read_b128 v[104:107], %4 offset:8192\n\t" \
  "v_exp_f32_e32 v64, v64\n\t" \
  "ds_read_b128 v[108:111], %4 offset:9216\n\t" \
  "v_exp_f32_e32 v65, v65\n\t" \
  "v_exp_f32_e32 v66, v66\n\t" \
  "v_cvt_pk_bf16_f32 v96, v64, v65\n\t" \
  "v_mfma_f32_32x32x16_bf16 v[80:95], v[116:119], %11, v[80:95]\n\t" \
  "v_exp_f32_e32 v67, v67\n\t" \
  "v_exp_f32_e32 v68, v68\n\t" \
  "v_cvt_pk_bf16_f32 v97, v66, v67\n\t" \
  "v_exp_f32_e32 v69, v69\n\t" \
  "v_smfmac_f32_16x16x64_bf16 %3, %12, v[136:143], %17\n\t" \
  "v_exp_f32_e32 v70, v70\n\t" \
  "v_cvt_pk_bf16_f32 v98, v68, v69\n\t" \
  "v_mfma_f32_32x32x16_bf16 %1, v[124:127], v[140:143], %1\n\t" \
  "v_exp_f32_e32 v71, v71\n\t" \
  "v_exp_f32_e32 v72, v72\n\t" \
  "v_cvt_pk_bf16_f32 v99, v70, v71\n\t" \
  "v_exp_f32_e32 v73, v73\n\t" \
  "v_exp_f32_e32 v74, v74\n\t" \
  "v_mfma_f32_32x32x16_bf16 %0, v[128:131], v[96:99], %0\n\t" \
  "v_cvt_pk_bf16_f32 v100, v72, v73\n\t" \
  "v_exp_f32_e32 v75, v75\n\t" \
  "v_exp_f32_e32 v76, v76\n\t" \
  "v_cvt_pk_bf16_f32 v101, v74, v75\n\t" \
  "v_exp_f32_e32 v77, v77\n\t" \
  "v_exp_f32_e32 v78, v78\n\t" \
  "s_waitcnt lgkmcnt(0)\n\t" \
  "v_exp_f32_e32 v79, v79\n\t" \
  "v_cvt_pk_bf16_f32 v102, v76, v77\n\t" \
  "v_cvt_pk_bf16_f32 v103, v78, v79\n\t" \
  "v_mfma_f32_32x32x16_bf16 v[64:79], v[104:107], %8, 0\n\t" \
  "ds_read_b128 v[120:123], %4 offset:10240\n\t" \
  "v_exp_f32_e32 v80, v80\n\t" \
  "ds_read_b128 v[124:127], %4 offset:11264\n\t" \
  "v_exp_f32_e32 v81, v81\n\t" \
  "v_exp_f32_e32 v82, v82\n\t" \
  "v_cvt_pk_bf16_f32 v136, v80, v81\n\t" \
  "v_mfma_f32_32x32x16_bf16 v[64:79], v[108:111], %9, v[64:79]\n\t" \
  "v_exp_f32_e32 v83, v83\n\t" \
  "v_exp_f32_e32 v84, v84\n\t" \
  "v_cvt_pk_bf16_f32 v137, v82, v83\n\t" \
  "v_exp_f32_e32 v85, v85\n\t" \
  "v_smfmac_f32_16x16x64_bf16 %2, %12, v[96:103], %17\n\t" \
  "v_exp_f32_e32 v86, v86\n\t" \
  "v_cvt_pk_bf16_f32 v138, v84, v85\n\t" \
  "v_mfma_f32_32x32x16_bf16 %0, v[132:135], v[100:103], %0\n\t" \
  "v_exp_f32_e32 v87, v87\n\t" \
  "v_exp_f32_e32 v88, v88\n\t" \
  "v_cvt_pk_bf16_f32 v139, v86, v87\n\t" \
  "v_exp_f32_e32 v89, v89\n\t" \
  "v_exp_f32_e32 v90, v90\n\t" \
  "v_mfma_f32_32x32x16_bf16 %1, v[128:131], v[136:139], %1\n\t" \
  "v_cvt_pk_bf16_f32 v140, v88, v89\n\t" \
  "v_exp_f32_e32 v91, v91\n\t" \
  "v_exp_f32_e32 v92, v92\n\t" \
  "v_cvt_pk_bf16_f32 v141, v90, v91\n\t" \
  "v_exp_f32_e32 v93, v93\n\t" \
  "v_exp_f32_e32 v94, v94\n\t" \
  "s_waitcnt lgkmcnt(0)\n\t" \
  "v_exp_f32_e32 v95, v95\n\t" \
  "v_cvt_pk_bf16_f32 v142, v92, v93\n\t" \
  "v_cvt_pk_bf16_f32 v143, v94, v95\n\t" \
  "v_mfma_f32_32x32x16_bf16 v[80:95], v[104:107], %10, 0\n\t" \
  "ds_read_b128 v[112:115], %4 offset:12288\n\t" \
  "v_exp_f32_e32 v64, v64\n\t" \
  "ds_read_b128 v[116:119], %4 offset:13312\n\t" \
  "v_exp_f32_e32 v65, v65\n\t" \
  "v_exp_f32_e32 v66, v66\n\t" \
  "v_cvt_pk_bf16_f32 v96, v64, v65\n\t" \
  "v_mfma_f32_32x32x16_bf16 v[80:95], v[108:111], %11, v[80:95]\n\t" \
  "v_exp_f32_e32 v67, v67\n\t" \
  "v_exp_f32_e32 v68, v68\n\t" \
  "v_cvt_pk_bf16_f32 v97, v66, v67\n\t" \
  "v_exp_f32_e32 v69, v69\n\t" \
  "v_smfmac_f32_16x16x64_bf16 %3, %12, v[136:143], %17\n\t" \
  "v_exp_f32_e32 v70, v70\n\t" \
  "v_cvt_pk_bf16_f32 v98, v68, v69\n\t" \
  "v_mfma_f32_32x32x16_bf16 %1, v[132:135], v[140:143], %1\n\t" \
  "v_exp_f32_e32 v71, v71\n\t" \
  "v_exp_f32_e32 v72, v72\n\t" \
  "v_cvt_pk_bf16_f32 v99, v70, v71\n\t" \
  "v_exp_f32_e32 v73, v73\n\t" \
  "v_exp_f32_e32 v74, v74\n\t" \
  "v_mfma_f32_32x32x16_bf16 %0, v[120:123], v[96:99], %0\n\t" \
  "v_cvt_pk_bf16_f32 v100, v72, v73\n\t" \
  "v_exp_f32_e32 v75, v75\n\t" \
  "v_exp_f32_e32 v76, v76\n\t" \
  "v_cvt_pk_bf16_f32 v101, v74, v75\n\t" \
  "v_exp_f32_e32 v77, v77\n\t" \
  "v_exp_f32_e32 v78, v78\n\t" \
  "s_waitcnt lgkmcnt(0)\n\t" \
  "v_exp_f32_e32 v79, v79\n\t" \
  "v_cvt_pk_bf16_f32 v102, v76, v77\n\t" \
  "v_cvt_pk_bf16_f32 v103, v78, v79\n\t" \
  "v_mfma_f32_32x32x16_bf16 v[64:79], v[112:115], %8, 0\n\t" \
  "ds_read_b128 v[128:131], %4 offset:14336\n\t" \
  "v_exp_f32_e32 v80, v80\n\t" \
  "ds_read_b128 v[132:135], %4 offset:15360\n\t" \
  "v_exp_f32_e32 v81, v81\n\t" \
  "v_exp_f32_e32 v82, v82\n\t" \
  "v_cvt_pk_bf16_f32 v136, v80, v81\n\t" \
  "v_mfma_f32_32x32x16_bf16 v[64:79], v[116:119], %9, v[64:79]\n\t" \
  "v_exp_f32_e32 v83, v83\n\t" \
  "v_exp_f32_e32 v84, v84\n\t" \
  "v_cvt_pk_bf16_f32 v137, v82, v83\n\t" \
  "v_exp_f32_e32 v85, v85\n\t" \
  "v_smfmac_f32_16x16x64_bf16 %2, %12, v[96:103], %17\n\t" \
  "v_exp_f32_e32 v86, v86\n\t" \
  "v_cvt_pk_bf16_f32 v138, v84, v85\n\t" \
  "v_mfma_f32_32x32x16_bf16 %0, v[124:127], v[100:103], %0\n\t" \
  "v_exp_f32_e32 v87, v87\n\t" \
  "v_exp_f32_e32 v88, v88\n\t" \
  "v_cvt_pk_bf16_f32 v139, v86, v87\n\t" \
  "v_exp_f32_e32 v89, v89\n\t" \
  "v_exp_f32_e32 v90, v90\n\t" \
  "v_mfma_f32_32x32x16_bf16 %1, v[120:123], v[136:139], %1\n\t" \
  "v_cvt_pk_bf16_f32 v140, v88, v89\n\t" \
  "v_exp_f32_e32 v91, v91\n\t" \
  "v_exp_f32_e32 v92, v92\n\t" \
  "v_cvt_pk_bf16_f32 v141, v90, v91\n\t" \
  "v_exp_f32_e32 v93, v93\n\t" \
  "v_exp_f32_e32 v94, v94\n\t" \
  "s_waitcnt lgkmcnt(0)\n\t" \
  "v_exp_f32_e32 v95, v95\n\t" \
  "v_cvt_pk_bf16_f32 v142, v92, v93\n\t" \
  "v_cvt_pk_bf16_f32 v143, v94, v95\n\t" \
  "v_mfma_f32_32x32x16_bf16 v[80:95], v[112:115], %10, 0\n\t" \
  "ds_read_b128 v[104:107], %4 offset:16384\n\t" \
  "v_exp_f32_e32 v64, v64\n\t" \
  "ds_read_b128 v[108:111], %4 offset:17408\n\t" \
  "v_exp_f32_e32 v65, v65\n\t" \
  "v_exp_f32_e32 v66, v66\n\t" \
  "v_cvt_pk_bf16_f32 v96, v64, v65\n\t" \
  "v_mfma_f32_32x32x16_bf16 v[80:95], v[116:119], %11, v[80:95]\n\t" \
  "v_exp_f32_e32 v67, v67\n\t" \
  "v_exp_f32_e32 v68, v68\n\t" \
  "v_cvt_pk_bf16_f32 v97, v66, v67\n\t" \
  "v_exp_f32_e32 v69, v69\n\t" \
  "v_smfmac_f32_16x16x64_bf16 %3, %12, v[136:143], %17\n\t" \
  "v_exp_f32_e32 v70, v70\n\t" \
  "v_cvt_pk_bf16_f32 v98, v68, v69\n\t" \
  "v_mfma_f32_32x32x16_bf16 %1, v[124:127], v[140:143], %1\n\t" \
  "v_exp_f32_e32 v71, v71\n\t" \
  "v_exp_f32_e32 v72, v72\n\t" \
  "v_cvt_pk_bf16_f32 v99, v70, v71\n\t" \
  "v_exp_f32_e32 v73, v73\n\t" \
  "v_exp_f32_e32 v74, v74\n\t" \
  "v_mfma_f32_32x32x16_bf16 %0, v[128:131], v[96:99], %0\n\t" \
  "v_cvt_pk_bf16_f32 v100, v72, v73\n\t" \
  "v_exp_f32_e32 v75, v75\n\t" \
  "v_exp_f32_e32 v76, v76\n\t" \
  "v_cvt_pk_bf16_f32 v101, v74, v75\n\t" \
  "v_exp_f32_e32 v77, v77\n\t" \
  "v_exp_f32_e32 v78, v78\n\t" \
  "s_waitcnt lgkmcnt(0)\n\t" \
  "v_exp_f32_e32 v79, v79\n\t" \
  "v_cvt_pk_bf16_f32 v102, v76, v77\n\t" \
  "v_cvt_pk_bf16_f32 v103, v78, v79\n\t" \
  "v_mfma_f32_32x32x16_bf16 v[64:79], v[104:107], %8, 0\n\t" \
  "ds_read_b128 v[120:123], %4 offset:18432\n\t" \
  "v_exp_f32_e32 v80, v80\n\t" \
  "ds_read_b128 v[124:127], %4 offset:19456\n\t" \
  "v_exp_f32_e32 v81, v81\n\t" \
  "v_exp_f32_e32 v82, v82\n\t" \
  "v_cvt_pk_bf16_f32 v136, v80, v81\n\t" \
  "v_mfma_f32_32x32x16_bf16 v[64:79], v[108:111], %9, v[64:79]\n\t" \
  "v_exp_f32_e32 v83, v83\n\t" \
  "v_exp_f32_e32 v84, v84\n\t" \
  "v_cvt_pk_bf16_f32 v137, v82, v83\n\t" \
  "v_exp_f32_e32 v85, v85\n\t" \
  "v_smfmac_f32_16x16x64_bf16 %2, %12, v[96:103], %17\n\t" \
  "v_exp_f32_e32 v86, v86\n\t" \
  "v_cvt_pk_bf16_f32 v138, v84, v85\n\t" \
  "v_mfma_f32_32x32x16_bf16 %0, v[132:135], v[100:103], %0\n\t" \
  "v_exp_f32_e32 v87, v87\n\t" \
  "v_exp_f32_e32 v88, v88\n\t" \
  "v_cvt_pk_bf16_f32 v139, v86, v87\n\t" \
  "v_exp_f32_e32 v89, v89\n\t" \
  "v_exp_f32_e32 v90, v90\n\t" \
  "v_mfma_f32_32x32x16_bf16 %1, v[128:131], v[136:139], %1\n\t" \
  "v_cvt_pk_bf16_f32 v140, v88, v89\n\t" \
  "v_exp_f32_e32 v91, v91\n\t" \
  "v_exp_f32_e32 v92, v92\n\t" \
  "v_cvt_pk_bf16_f32 v141, v90, v91\n\t" \
  "v_exp_f32_e32 v93, v93\n\t" \
  "v_exp_f32_e32 v94, v94\n\t" \
  "s_waitcnt lgkmcnt(0)\n\t" \
  "v_exp_f32_e32 v95, v95\n\t" \
  "v_cvt_pk_bf16_f32 v142, v92, v93\n\t" \
  "v_cvt_pk_bf16_f32 v143, v94, v95\n\t" \
  "v_mfma_f32_32x32x16_bf16 v[80:95], v[104:107], %10, 0\n\t" \
  "ds_read_b128 v[112:115], %4 offset:20480\n\t" \
  "v_exp_f32_e32 v64, v64\n\t" \
  "ds_read_b128 v[116:119], %4 offset:21504\n\t" \
  "v_exp_f32_e32 v65, v65\n\t" \
  "v_exp_f32_e32 v66, v66\n\t" \
  "v_cvt_pk_bf16_f32 v96, v64, v65\n\t" \
  "v_mfma_f32_32x32x16_bf16 v[80:95], v[108:111], %11, v[80:95]\n\t" \
  "v_exp_f32_e32 v67, v67\n\t" \
  "v_exp_f32_e32 v68, v68\n\t" \
  "v_cvt_pk_bf16_f32 v97, v66, v67\n\t" \
  "v_exp_f32_e32 v69, v69\n\t" \
  "v_smfmac_f32_16x16x64_bf16 %3, %12, v[136:143], %17\n\t" \
  "v_exp_f32_e32 v70, v70\n\t" \
  "v_cvt_pk_bf16_f32 v98, v68, v69\n\t" \
  "v_mfma_f32_32x32x16_bf16 %1, v[132:135], v[140:143], %1\n\t" \
  "v_exp_f32_e32 v71, v71\n\t" \
  "v_exp_f32_e32 v72, v72\n\t" \
  "v_cvt_pk_bf16_f32 v99, v70, v71\n\t" \
  "v_exp_f32_e32 v73, v73\n\t" \
  "v_exp_f32_e32 v74, v74\n\t" \
  "v_mfma_f32_32x32x16_bf16 %0, v[120:123], v[96:99], %0\n\t" \
  "v_cvt_pk_bf16_f32 v100, v72, v73\n\t" \
  "v_exp_f32_e32 v75, v75\n\t" \
  "v_exp_f32_e32 v76, v76\n\t" \
  "v_cvt_pk_bf16_f32 v101, v74, v75\n\t" \
  "v_exp_f32_e32 v77, v77\n\t" \
  "v_exp_f32_e32 v78, v78\n\t" \
  "s_waitcnt lgkmcnt(0)\n\t" \
  "v_exp_f32_e32 v79, v79\n\t" \
  "v_cvt_pk_bf16_f32 v102, v76, v77\n\t" \
  "v_cvt_pk_bf16_f32 v103, v78, v79\n\t" \
  "v_mfma_f32_32x32x16_bf16 v[64:79], v[112:115], %8, 0\n\t" \
  "ds_read_b128 v[128:131], %4 offset:22528\n\t" \
  "v_exp_f32_e32 v80, v80\n\t" \
  "ds_read_b128 v[132:135], %4 offset:23552\n\t" \
  "v_exp_f32_e32 v81, v81\n\t" \
  "v_exp_f32_e32 v82, v82\n\t" \
  "v_cvt_pk_bf16_f32 v136, v80, v81\n\t" \
  "v_mfma_f32_32x32x16_bf16 v[64:79], v[116:119], %9, v[64:79]\n\t" \
  "v_exp_f32_e32 v83, v83\n\t" \
  "v_exp_f32_e32 v84, v84\n\t" \
  "v_cvt_pk_bf16_f32 v137, v82, v83\n\t" \
  "v_exp_f32_e32 v85, v85\n\t" \
  "v_smfmac_f32_16x16x64_bf16 %2, %12, v[96:103], %17\n\t" \
  "v_exp_f32_e32 v86, v86\n\t" \
  "v_cvt_pk_bf16_f32 v138, v84, v85\n\t" \
  "v_mfma_f32_32x32x16_bf16 %0, v[124:127], v[100:103], %0\n\t" \
  "v_exp_f32_e32 v87, v87\n\t" \
  "v_exp_f32_e32 v88, v88\n\t" \
  "v_cvt_pk_bf16_f32 v139, v86, v87\n\t" \
  "v_exp_f32_e32 v89, v89\n\t" \
  "v_exp_f32_e32 v90, v90\n\t" \
  "v_mfma_f32_32x32x16_bf16 %1, v[120:123], v[136:139], %1\n\t" \
  "v_cvt_pk_bf16_f32 v140, v88, v89\n\t" \
  "v_exp_f32_e32 v91, v91\n\t" \
  "v_exp_f32_e32 v92, v92\n\t" \
  "v_cvt_pk_bf16_f32 v141, v90, v91\n\t" \
  "v_exp_f32_e32 v93, v93\n\t" \
  "v_exp_f32_e32 v94, v94\n\t" \
  "s_waitcnt lgkmcnt(0)\n\t" \
  "v_exp_f32_e32 v95, v95\n\t" \
  "v_cvt_pk_bf16_f32 v142, v92, v93\n\t" \
  "v_cvt_pk_bf16_f32 v143, v94, v95\n\t" \
  "v_mfma_f32_32x32x16_bf16 v[80:95], v[112:115], %10, 0\n\t" \
  "ds_read_b128 v[104:107], %4 offset:24576\n\t" \
  "v_exp_f32_e32 v64, v64\n\t" \
  "ds_read_b128 v[108:111], %4 offset:25600\n\t" \
  "v_exp_f32_e32 v65, v65\n\t" \
  "v_exp_f32_e32 v66, v66\n\t" \
  "v_cvt_pk_bf16_f32 v96, v64, v65\n\t" \
  "v_mfma_f32_32x32x16_bf16 v[80:95], v[116:119], %11, v[80:95]\n\t" \
  "v_exp_f32_e32 v67, v67\n\t" \
  "v_exp_f32_e32 v68, v68\n\t" \
  "v_cvt_pk_bf16_f32 v97, v66, v67\n\t" \
  "v_exp_f32_e32 v69, v69\n\t" \
  "v_smfmac_f32_16x16x64_bf16 %3, %12, v[136:143], %17\n\t" \
  "v_exp_f32_e32 v70, v70\n\t" \
  "v_cvt_pk_bf16_f32 v98, v68, v69\n\t" \
  "v_mfma_f32_32x32x16_bf16 %1, v[124:127], v[140:143], %1\n\t" \
  "v_exp_f32_e32 v71, v71\n\t" \
  "v_exp_f32_e32 v72, v72\n\t" \
  "v_cvt_pk_bf16_f32 v99, v70, v71\n\t" \
  "v_exp_f32_e32 v73, v73\n\t" \
  "v_exp_f32_e32 v74, v74\n\t" \
  "v_mfma_f32_32x32x16_bf16 %0, v[128:131], v[96:99], %0\n\t" \
  "v_cvt_pk_bf16_f32 v100, v72, v73\n\t" \
  "v_exp_f32_e32 v75, v75\n\t" \
  "v_exp_f32_e32 v76, v76\n\t" \
  "v_cvt_pk_bf16_f32 v101, v74, v75\n\t" \
  "v_exp_f32_e32 v77, v77\n\t" \
  "v_exp_f32_e32 v78, v78\n\t" \
  "s_waitcnt lgkmcnt(0)\n\t" \
  "v_exp_f32_e32 v79, v79\n\t" \
  "v_cvt_pk_bf16_f32 v102, v76, v77\n\t" \
  "v_cvt_pk_bf16_f32 v103, v78, v79\n\t" \
  "v_mfma_f32_32x32x16_bf16 v[64:79], v[104:107], %8, 0\n\t" \
  "ds_read_b128 v[120:123], %4 offset:26624\n\t" \
  "v_exp_f32_e32 v80, v80\n\t" \
  "ds_read_b128 v[124:127], %4 offset:27648\n\t" \
  "v_exp_f32_e32 v81, v81\n\t" \
  "v_exp_f32_e32 v82, v82\n\t" \
  "v_cvt_pk_bf16_f32 v136, v80, v81\n\t" \
  "v_mfma_f32_32x32x16_bf16 v[64:79], v[108:111], %9, v[64:79]\n\t" \
  "v_exp_f32_e32 v83, v83\n\t" \
  "v_exp_f32_e32 v84, v84\n\t" \
  "v_cvt_pk_bf16_f32 v137, v82, v83\n\t" \
  "v_exp_f32_e32 v85, v85\n\t" \
  "v_smfmac_f32_16x16x64_bf16 %2, %12, v[96:103], %17\n\t" \
  "v_exp_f32_e32 v86, v86\n\t" \
  "v_cvt_pk_bf16_f32 v138, v84, v85\n\t" \
  "v_mfma_f32_32x32x16_bf16 %0, v[132:135], v[100:103], %0\n\t" \
  "v_exp_f32_e32 v87, v87\n\t" \
  "v_exp_f32_e32 v88, v88\n\t" \
  "v_cvt_pk_bf16_f32 v139, v86, v87\n\t" \
  "v_exp_f32_e32 v89, v89\n\t" \
  "v_exp_f32_e32 v90, v90\n\t" \
  "v_mfma_f32_32x32x16_bf16 %1, v[128:131], v[136:139], %1\n\t" \
  "v_cvt_pk_bf16_f32 v140, v88, v89\n\t" \
  "v_exp_f32_e32 v91, v91\n\t" \
  "v_exp_f32_e32 v92, v92\n\t" \
  "v_cvt_pk_bf16_f32 v141, v90, v91\n\t" \
  "v_exp_f32_e32 v93, v93\n\t" \
  "v_exp_f32_e32 v94, v94\n\t" \
  "s_waitcnt lgkmcnt(0)\n\t" \
  "v_exp_f32_e32 v95, v95\n\t" \
  "v_cvt_pk_bf16_f32 v142, v92, v93\n\t" \
  "v_cvt_pk_bf16_f32 v143, v94, v95\n\t" \
  "v_mfma_f32_32x32x16_bf16 v[80:95], v[104:107], %10, 0\n\t" \
  "ds_read_b128 v[112:115], %4 offset:28672\n\t" \
  "v_exp_f32_e32 v64, v64\n\t" \
  "ds_read_b128 v[116:119], %4 offset:29696\n\t" \
  "v_exp_f32_e32 v65, v65\n\t" \
  "v_exp_f32_e32 v66, v66\n\t" \
  "v_cvt_pk_bf16_f32 v96, v64, v65\n\t" \
  "v_mfma_f32_32x32x16_bf16 v[80:95], v[108:111], %11, v[80:95]\n\t" \
  "v_exp_f32_e32 v67, v67\n\t" \
  "v_exp_f32_e32 v68, v68\n\t" \
  "v_cvt_pk_bf16_f32 v97, v66, v67\n\t" \
  "v_exp_f32_e32 v69, v69\n\t" \
  "v_smfmac_f32_16x16x64_bf16 %3, %12, v[136:143], %17\n\t" \
  "v_exp_f32_e32 v70, v70\n\t" \
  "v_cvt_pk_bf16_f32 v98, v68, v69\n\t" \
  "v_mfma_f32_32x32x16_bf16 %1, v[132:135], v[140:143], %1\n\t" \
  "v_exp_f32_e32 v71, v71\n\t" \
  "v_exp_f32_e32 v72, v72\n\t" \
  "v_cvt_pk_bf16_f32 v99, v70, v71\n\t" \
  "v_exp_f32_e32 v73, v73\n\t" \
  "v_exp_f32_e32 v74, v74\n\t" \
  "v_mfma_f32_32x32x16_bf16 %0, v[120:123], v[96:99], %0\n\t" \
  "v_cvt_pk_bf16_f32 v100, v72, v73\n\t" \
  "v_exp_f32_e32 v75, v75\n\t" \
  "v_exp_f32_e32 v76, v76\n\t" \
  "v_cvt_pk_bf16_f32 v101, v74, v75\n\t" \
  "v_exp_f32_e32 v77, v77\n\t" \
  "v_exp_f32_e32 v78, v78\n\t" \
  "s_waitcnt lgkmcnt(0)\n\t" \
  "v_exp_f32_e32 v79, v79\n\t" \
  "v_cvt_pk_bf16_f32 v102, v76, v77\n\t" \
  "v_cvt_pk_bf16_f32 v103, v78, v79\n\t" \
  "v_mfma_f32_32x32x16_bf16 v[64:79], v[112:115], %8, 0\n\t" \
  "ds_read_b128 v[128:131], %4 offset:30720\n\t" \
  "v_exp_f32_e32 v80, v80\n\t" \
  "ds_read_b128 v[132:135], %4 offset:31744\n\t" \
  "v_exp_f32_e32 v81, v81\n\t" \
  "v_exp_f32_e32 v82, v82\n\t" \
  "v_cvt_pk_bf16_f32 v136, v80, v81\n\t" \
  "v_mfma_f32_32x32x16_bf16 v[64:79], v[116:119], %9, v[64:79]\n\t" \
  "v_exp_f32_e32 v83, v83\n\t" \
  "v_exp_f32_e32 v84, v84\n\t" \
  "v_cvt_pk_bf16_f32 v137, v82, v83\n\t" \
  "v_exp_f32_e32 v85, v85\n\t" \
  "v_smfmac_f32_16x16x64_bf16 %2, %12, v[96:103], %17\n\t" \
  "v_exp_f32_e32 v86, v86\n\t" \
  "v_cvt_pk_bf16_f32 v138, v84, v85\n\t" \
  "v_mfma_f32_32x32x16_bf16 %0, v[124:127], v[100:103], %0\n\t" \
  "v_exp_f32_e32 v87, v87\n\t" \
  "v_exp_f32_e32 v88, v88\n\t" \
  "v_cvt_pk_bf16_f32 v139, v86, v87\n\t" \
  "v_exp_f32_e32 v89, v89\n\t" \
  "v_exp_f32_e32 v90, v90\n\t" \
  "v_mfma_f32_32x32x16_bf16 %1, v[120:123], v[136:139], %1\n\t" \
  "v_cvt_pk_bf16_f32 v140, v88, v89\n\t" \
  "v_exp_f32_e32 v91, v91\n\t" \
  "v_exp_f32_e32 v92, v92\n\t" \
  "v_cvt_pk_bf16_f32 v141, v90, v91\n\t" \
  "v_exp_f32_e32 v93, v93\n\t" \
  "v_exp_f32_e32 v94, v94\n\t" \
  "s_waitcnt lgkmcnt(0)\n\t" \
  "v_exp_f32_e32 v95, v95\n\t" \
  "v_cvt_pk_bf16_f32 v142, v92, v93\n\t" \
  "v_cvt_pk_bf16_f32 v143, v94, v95\n\t" \
  "v_mfma_f32_32x32x16_bf16 v[80:95], v[112:115], %10, 0\n\t" \
  "ds_read_b128 v[104:107], %5 offset:0\n\t" \
  "v_exp_f32_e32 v64, v64\n\t" \
  "ds_read_b128 v[108:111], %5 offset:1024\n\t" \
  "v_exp_f32_e32 v65, v65\n\t" \
  "v_exp_f32_e32 v66, v66\n\t" \
  "v_cvt_pk_bf16_f32 v96, v64, v65\n\t" \
  "v_mfma_f32_32x32x16_bf16 v[80:95], v[116:119], %11, v[80:95]\n\t" \
  "v_exp_f32_e32 v67, v67\n\t" \
  "v_exp_f32_e32 v68, v68\n\t" \
  "v_cvt_pk_bf16_f32 v97, v66, v67\n\t" \
  "v_exp_f32_e32 v69, v69\n\t" \
  "v_smfmac_f32_16x16x64_bf16 %3, %12, v[136:143], %17\n\t" \
  "v_exp_f32_e32 v70, v70\n\t" \
  "v_cvt_pk_bf16_f32 v98, v68, v69\n\t" \
  "v_mfma_f32_32x32x16_bf16 %1, v[124:127], v[140:143], %1\n\t" \
  "v_exp_f32_e32 v71, v71\n\t" \
  "v_exp_f32_e32 v72, v72\n\t" \
  "v_cvt_pk_bf16_f32 v99, v70, v71\n\t" \
  "v_exp_f32_e32 v73, v73\n\t" \
  "v_exp_f32_e32 v74, v74\n\t" \
  "v_mfma_f32_32x32x16_bf16 %0, v[128:131], v[96:99], %0\n\t" \
  "v_cvt_pk_bf16_f32 v100, v72, v73\n\t" \
  "v_exp_f32_e32 v75, v75\n\t" \
  "v_exp_f32_e32 v76, v76\n\t" \
  "v_cvt_pk_bf16_f32 v101, v74, v75\n\t" \
  "v_exp_f32_e32 v77, v77\n\t" \
  "v_exp_f32_e32 v78, v78\n\t" \
  "s_waitcnt lgkmcnt(0)\n\t" \
  "v_exp_f32_e32 v79, v79\n\t" \
  "v_cvt_pk_bf16_f32 v102, v76, v77\n\t" \
  "v_cvt_pk_bf16_f32 v103, v78, v79\n\t" \
  "v_mfma_f32_32x32x16_bf16 v[64:79], v[104:107], %8, 0\n\t" \
  "ds_read_b128 v[120:123], %5 offset:2048\n\t" \
  "v_exp_f32_e32 v80, v80\n\t" \
  "ds_read_b128 v[124:127], %5 offset:3072\n\t" \
  "v_exp_f32_e32 v81, v81\n\t" \
  "v_exp_f32_e32 v82, v82\n\t" \
  "v_cvt_pk_bf16_f32 v136, v80, v81\n\t" \
  "v_mfma_f32_32x32x16_bf16 v[64:79], v[108:111], %9, v[64:79]\n\t" \
  "v_exp_f32_e32 v83, v83\n\t" \
  "v_exp_f32_e32 v84, v84\n\t" \
  "v_cvt_pk_bf16_f32 v137, v82, v83\n\t" \
  "v_exp_f32_e32 v85, v85\n\t" \
  "v_smfmac_f32_16x16x64_bf16 %2, %12, v[96:103], %17\n\t" \
  "v_exp_f32_e32 v86, v86\n\t" \
  "v_cvt_pk_bf16_f32 v138, v84, v85\n\t" \
  "v_mfma_f32_32x32x16_bf16 %0, v[132:135], v[100:103], %0\n\t" \
  "v_exp_f32_e32 v87, v87\n\t" \
  "v_exp_f32_e32 v88, v88\n\t" \
  "v_cvt_pk_bf16_f32 v139, v86, v87\n\t" \
  "v_exp_f32_e32 v89, v89\n\t" \
  "v_exp_f32_e32 v90, v90\n\t" \
  "v_mfma_f32_32x32x16_bf16 %1, v[128:131], v[136:139], %1\n\t" \
  "v_cvt_pk_bf16_f32 v140, v88, v89\n\t" \
  "v_exp_f32_e32 v91, v91\n\t" \
  "v_exp_f32_e32 v92, v92\n\t" \
  "v_cvt_pk_bf16_f32 v141, v90, v91\n\t" \
  "v_exp_f32_e32 v93, v93\n\t" \
  "v_exp_f32_e32 v94, v94\n\t" \
  "s_waitcnt lgkmcnt(0)\n\t" \
  "v_exp_f32_e32 v95, v95\n\t" \
  "v_cvt_pk_bf16_f32 v142, v92, v93\n\t" \
  "v_cvt_pk_bf16_f32 v143, v94, v95\n\t" \
  "v_mov_b32 %4, %5\n\t" \
  "v_mov_b32 %5, %16\n\t" \
  "v_add_u32 %7, 1, %7\n\t" \
  "ds_write_b32 %15, %7\n\t" \
  "s_cbranch_scc0 LB_%=\n\t" \
  "s_nop 1\n\t" \
  "v_smfmac_f32_16x16x64_bf16 %3, %12, v[136:143], %17\n\t" \
  "v_mfma_f32_32x32x16_bf16 %1, v[132:135], v[140:143], %1\n\t" \
  "s_nop 15\n\t" \
  "s_nop 3\n\t" \
  ""
#define ATTN_CLOB "v64", "v65", "v66", "v67", "v68", "v69", "v70", "v71", "v72", "v73", "v74", "v75", "v76", "v77", "v78", "v79", "v80", "v81", "v82", "v83", "v84", "v85", "v86", "v87", "v88", "v89", "v90", "v91", "v92", "v93", "v94", "v95", "v96", "v97", "v98", "v99", "v100", "v101", "v102", "v103", "v104", "v105", "v106", "v107", "v108", "v109", "v110", "v111", "v112", "v113", "v114", "v115", "v116", "v117", "v118", "v119", "v120", "v121", "v122", "v123", "v124", "v125", "v126", "v127", "v128", "v129", "v130", "v131", "v132", "v133", "v134", "v135", "v136", "v137", "v138", "v139", "v140", "v141", "v142", "v143", "vcc", "scc", "memory"

constexpr int NPRE = 7;
constexpr int GEND = 18;
constexpr int ROT = 4;
constexpr int CODE_PF = 65536;
#define GAS __attribute__((address_space(1)))
typedef unsigned long long u64;
typedef GAS u64 gu64;

template <int SC1> DI void dmap(unsigned voff, u32x4 srd, unsigned soff, unsigned base, int imm) {
  if constexpr (!SC1) asm volatile("s_add_u32 m0, %3, %4\n\ts_nop 0\n\tbuffer_load_dwordx4 %0, %1, %2 offen lds" :: "v"(voff), "s"(srd), "s"(soff), "s"(base), "i"(imm) : "memory", "scc");
  else asm volatile("s_add_u32 m0, %3, %4\n\ts_nop 0\n\tbuffer_load_dwordx4 %0, %1, %2 offen sc1 lds" :: "v"(voff), "s"(srd), "s"(soff), "s"(base), "i"(imm) : "memory", "scc");
}
template <int N, class F> DI void static_for(F&& f) { if constexpr (N > 0) { static_for<N - 1>(f); f(std::integral_constant<int, N - 1>{}); } }
DI void bar() { asm volatile("s_waitcnt lgkmcnt(0)\n\ts_barrier" ::: "memory"); }
template <int N> DI void wait_vm() { asm volatile("s_waitcnt vmcnt(%0)" :: "i"(N) : "memory"); }
DI lds_cptr opaque(lds_cptr p) { unsigned a = (unsigned)(uintptr_t)p; asm volatile("" : "+v"(a)); return (lds_cptr)(uintptr_t)a; }
DI void st16(__amdgpu_buffer_rsrc_t r, unsigned off, bf16x8 v) { __builtin_amdgcn_raw_buffer_store_b128(__builtin_bit_cast(u32x4, v), r, off, 0, 16); }
DI void lds_wait_ge(lds_cptr f, unsigned n) {
#pragma unroll 1
  for (int i = 0; i < (1 << 22); ++i) { if ((unsigned)__builtin_amdgcn_readfirstlane(*(const volatile LAS unsigned*)f) >= n) break; __builtin_amdgcn_s_sleep(1); }
  asm volatile("" ::: "memory");
}
DI unsigned lds_arrive(lds_ptr f, int lane) {
  asm volatile("s_waitcnt lgkmcnt(0)" ::: "memory");
  unsigned o = 0;
  if (lane == 0) o = __hip_atomic_fetch_add((LAS unsigned*)f, 1u, __ATOMIC_RELAXED, __HIP_MEMORY_SCOPE_WORKGROUP);
  return __builtin_amdgcn_readfirstlane(o);
}
DI void poll16(const gu64* p, u64 tag, int lane) {
#pragma unroll 1
  for (int i = 0; i < (1 << 20); ++i) { const u64 v = __hip_atomic_load(p + (lane & 15) * 16, __ATOMIC_RELAXED, __HIP_MEMORY_SCOPE_AGENT); if (__all(v == tag)) break; __builtin_amdgcn_s_sleep(4); }
  __builtin_amdgcn_fence(__ATOMIC_ACQUIRE, "wavefront");
}
template <int CTRL> DI float dppf(float v) { return __uint_as_float(__builtin_amdgcn_update_dpp(0u, __float_as_uint(v), CTRL, 0xf, 0xf, true)); }
DI float other_half(float v) { const auto r = __builtin_amdgcn_permlane32_swap(__float_as_uint(v), __float_as_uint(v), false, false); return __uint_as_float(r[0] ^ r[1] ^ __float_as_uint(v)); }
DI float wave_max(float v) {
  v = fmaxf(v, dppf<0xB1>(v)); v = fmaxf(v, dppf<0x4E>(v)); v = fmaxf(v, dppf<0x141>(v)); v = fmaxf(v, dppf<0x140>(v));
  const auto r = __builtin_amdgcn_permlane16_swap(__float_as_uint(v), __float_as_uint(v), false, false);
  v = fmaxf(__uint_as_float(r[0]), __uint_as_float(r[1]));
  return fmaxf(v, other_half(v));
}
DI unsigned o_lane(int lane) { return ((lane >> 4) & 1) * 1024 + (lane >> 5) * 256 + (lane & 15) * 16; }
DI bf16x8 pack44(const f32x4& a, const f32x4& b) { const u32x4 p = {cvtpk(a[0], a[1]), cvtpk(a[2], a[3]), cvtpk(b[0], b[1]), cvtpk(b[2], b[3])}; return __builtin_bit_cast(bf16x8, p); }
DI float faddn(float a, float b) { float r; asm("v_add_f32_e32 %0, %1, %2\n\ts_nop 1" : "=v"(r) : "v"(a), "v"(b)); return r; }
DI float sc(float x) { asm("" : "+v"(x)); return x; }
DI f32x4 phi4(const f32x4& a) { return (f32x4){sc(phi1(sc(a[0]))), sc(phi1(sc(a[1]))), sc(phi1(sc(a[2]))), sc(phi1(sc(a[3])))}; }
DI f32x4 fadd4(const f32x4 a, const f32x4 b) { return (f32x4){fadd1(a[0], b[0]), fadd1(a[1], b[1]), fadd1(a[2], b[2]), fadd1(a[3], b[3])}; }
DI void set_tag(char* p, u64 tag, int lane) { if (lane == 0) __hip_atomic_store((gu64*)p, tag, __ATOMIC_RELAXED, __HIP_MEMORY_SCOPE_AGENT); }

constexpr unsigned WS_OAF = 0, WS_OBF = 4u << 20, WS_KVP = 8u << 20, WS_KVF = 10u << 20, WS_ZP = WS_KVF + (256u << 10), WS_KVR = WS_ZP + 65536, WS_FLG = WS_KVR + 65536,
                   F_EPOCH = 0, F_KV = 4096  , F_KVP = F_KV + 16384  , F_KVR = F_KVP + 32768  , F_O = F_KVR + 32768  ;
constexpr int L_P1 = 40960, P1S = 28672;
constexpr int GS = 40960;
constexpr int gslot(int s) { return (s < 4 ? s : (s - 4) % 3) * GS; }
constexpr int L_SP = 122880, L_FLG = 126976, L_RING = 131072, L_TA = 0, L_TB = GS, L_TC = 2 * GS, KVS = 8192;
constexpr int NIV = 20;
constexpr int L_KVX = 155648, L_P1F = L_KVX + 4096;
constexpr int kvslot(int st) { return st < NIV ? L_RING + (st & 3) * KVS : (st < 24 ? L_TA : (st < 28 ? L_TB : L_TC)) + (st & 3) * KVS; }
constexpr int x3slot(int s) { return ((s & 2) ? L_TC : L_TA) + (s & 1) * 16384; }
constexpr int w3slot(int s) { return ((s & 2) ? L_TB : L_RING) + (s & 1) * 16384; }
DI int kv_block(int hp) { return (hp & 1) ? -1 : hp >> 1; }
constexpr int p3ord(int s) { return (s & 8) + 2 * (s & 3) + (((s >> 2) & 1) ^ 1); }
constexpr unsigned p3mask(int gq) { unsigned m = 0; for (int i = 0; i < 4; ++i) m |= 1u << p3ord(4 * gq + i); return m; }
constexpr int LDS_BYTES = 163840;
enum { FL_KVR = 8, FL_Z = 12, FL_OA = 16, FL_OB = 20, FL_KVP = 24, FL_KVOK = 28, FL_TA = 32  , FL_KVPOK = 48, FL_OOK = 52, FL_KMAX = 56  , FL_TAG = 64  , FL_KVROK = 72 };

constexpr int NINT = NPRE + NIV;
constexpr int units_in(int j) { if (j < NPRE) return 6; const int n = j - NPRE, U = 96 - 6 * NPRE; return n >= GEND ? 0 : ((n + 1) * U) / GEND - (n * U) / GEND; }
struct Sched { int u0[NINT]; int nop[NINT]; int op[NINT][40]; int wait[NINT]; int cst[NINT]; int cwait[NINT]; };
constexpr Sched make_sched() {
  Sched S{};
  int iv[96] = {}; int u = 0;
  for (int j = 0; j < NINT; ++j) { S.u0[j] = u; for (int k = 0; k < units_in(j); ++k) iv[u++] = j; }
  int order[700] = {}; int cnt = 0;
  for (int s = 0; s < 3; ++s) for (int i = 0; i < 10; ++i) order[cnt++] = s * 16 + i;
  for (int j = 0; j < NINT; ++j) {
    const int n = j - NPRE;
    if (n >= 1 && n + 3 < NIV) for (int k = 0; k < 2; ++k) { S.op[j][S.nop[j]++] = 1000 + 2 * (n + 3) + k; }
    S.cst[j] = -1;
    for (int s = 3; s < 16; ++s) if ((s == 3 ? -1 : iv[6 * (s < 7 ? s - 4 : s - 3) + 5]) == j - 1) {
      if (j < NPRE - 1) S.cst[j] = s; else for (int i = 0; i < 10; ++i) S.op[j][S.nop[j]++] = s * 16 + i; }
    S.cwait[j] = 0; for (int k = 0; k <= j; ++k) if (S.cst[k] > j + 1) S.cwait[j] += 10;
    for (int t = 0; t < 3; ++t) if (iv[83 + 6 * t] + 1 == j) for (int k = 0; k < 8; ++k) S.op[j][S.nop[j]++] = 1000 + 40 + 8 * t + k;
    if (n == 13 || n == 15) S.op[j][S.nop[j]++] = 2000 + (n - 13) / 2;
    for (int k = 0; k < S.nop[j]; ++k) order[cnt++] = S.op[j][k];
    int last = -1;
    for (int i = 0; i < cnt; ++i) {
      const int c = order[i]; bool nd = false;
      if (c >= 2000) nd = false; else if (c >= 1000) nd = (n == NIV - 1) || ((c - 1000) / 2 <= n + 1); else nd = iv[6 * (c / 16)] <= j + 1;
      if (nd) last = i;
    }
    S.wait[j] = cnt - 1 - last;
  }
  return S;
}
constexpr Sched SCH = make_sched();
static_assert(GEND < NIV - 1 && NPRE >= 6 && NPRE <= 12, "the tail stages are loaded inside the barrier phase");

struct Args { const u16 *Bx, *Bwq, *Bwk, *Bwv; char* ws; const u16 *Ax, *Awqkv, *Awo, *Bwo; u16* out; };

DI void x_offsets(unsigned (&vx)[4], int g, int lane) {
#pragma unroll
  for (int i = 0; i < 4; ++i) { const int row = 32 * g + 8 * i + (lane >> 3); vx[i] = row * 2048 + (((lane & 7) ^ ((row >> 1) & 7)) << 4); }
}
DI void g_offsets(unsigned (&vg)[6], int g, int lane) {
#pragma unroll
  for (int i = 0; i < 6; ++i) { const int c = (g * 6 + i) * 64 + lane, k = c / 24, pos = c - 24 * k;
    vg[i] = k * 6144 + (pos >> 3) * 2048 + (((pos & 7) ^ ((k & 2) | ((k >> 1) & 4))) << 4); }
}
DI void issue_g(int s, int rot, const unsigned (&vx)[4], const unsigned (&vg)[6], u32x4 sxa, u32x4 swa, unsigned b4, unsigned b6) {
  const int kk = (s & ~(ROT - 1)) | ((s + rot) & (ROT - 1));
#pragma unroll
  for (int i = 0; i < 4; ++i) dmap<0>(vx[i], sxa, kk * 128, b4, gslot(s) + i * 1024);
#pragma unroll
  for (int i = 0; i < 6; ++i) dmap<0>(vg[i], swa, kk * 64 * 6144, b6, gslot(s) + 16384 + i * 1024);
}
template <class G0>
DI void p1_loader(char* smem, const Args& a, const int g, const int lane, const int rt, const int hp, const int rot, const unsigned (&vx)[4], const unsigned b1, const unsigned b2, const unsigned b4, G0&& g0) {
  const u32x4 sx = make_srd(a.Bx + (size_t)rt * 128 * 1024, 128 * 2048);
  const u32x4 sw = make_srd(a.Bwq + hp * 64, 1024 * 2048 - hp * 128);
  const u32x4 se = make_srd((kv_block(hp) & 1) ? a.Bwv : a.Bwk, kv_block(hp) < 0 ? 0 : 65536);
  const unsigned vw0 = (16 * g + (lane >> 3)) * 2048 + (((lane & 7) ^ ((lane >> 3) & 2)) << 4), vw1 = vw0 ^ 64, ve = g * 1024 + ((lane ^ ((lane >> 4) & 2)) << 4);
  constexpr int ND = 7;
  auto issue = [&](int s) {
    const int kk = (s & ~(ROT - 1)) | ((s + rot) & (ROT - 1));
    const int base = L_P1 + (s & 3) * P1S;
#pragma unroll
    for (int i = 0; i < 4; ++i) dmap<0>(vx[i], sx, kk * 128, b4, base + i * 1024);
#pragma unroll
    for (int i = 0; i < 2; ++i) dmap<0>(i ? vw1 : vw0, sw, (kk * 64 + 8 * i) * 2048, b2, base + 16384 + i * 1024);
    dmap<0>(ve, se, kk * 4096, b1, base + 24576);
  };
  issue(0); issue(1);
  wait_bar<ND>();
  issue(2); issue(3);
  static_for<15>([&](auto S) {
    constexpr int s = S, last = s + 3 < 15 ? s + 3 : 15;
    constexpr int S0 = 12, S1 = 14;
    wait_bar<ND*(last - (s + 1)) + (s > S0 ? 10 : 0) + (s > S1 ? 10 : 0)>();
    if constexpr (s + 4 < 16) issue(s + 4);
    if constexpr (s == S0) g0(0);
    if constexpr (s == S1) g0(1);
    if constexpr (s == 14) g0(2);
  });
}

DI void loader_role(char* smem, const Args& a, const int g, const int lane) {
  char* ws = a.ws;
  const int xcd = blockIdx.x & 7, idx = blockIdx.x >> 3;
  const int rt = (xcd >> 1) * 4 + (idx & 3), hp = (xcd & 1) * 8 + (idx >> 2), rot = (idx & 3) + (idx >> 2);
  const unsigned lds0 = (unsigned)(uintptr_t)smem;
  unsigned vx[4], vg[6];
  unsigned b1 = lds0 + g * 1024, b2 = lds0 + g * 2048, b4 = lds0 + g * 4096, b6 = lds0 + g * 6144;
  asm volatile("" : "+s"(b1), "+s"(b2), "+s"(b4), "+s"(b6));
  __builtin_amdgcn_s_setprio(2);
  x_offsets(vx, g, lane);
  const u32x4 sxa = make_srd(a.Ax + (size_t)rt * 128 * 1024, 128 * 2048);
  const u32x4 swa = make_srd(a.Awqkv + hp * 64, 1024 * 6144 - hp * 128);
  auto g0 = [&](int s) {
    if (s == 0) { int l3 = lane; asm volatile("" : "+v"(l3)); g_offsets(vg, g, l3); }
    issue_g(s, rot, vx, vg, sxa, swa, b4, b6);
  };
  p1_loader(smem, a, g, lane, rt, hp, rot, vx, b1, b2, b4, g0);
  int lane2 = lane; asm volatile("" : "+v"(lane2));
  const u32x4 skv = make_srd(ws + WS_KVF, 262144);
  const int br = (xcd & 1) ^ 1, ct = idx >> 2;
  const u32x4 so = make_srd((br ? a.Bwo : a.Awo) + ct * 128, 1024 * 2048 - ct * 256);
  const int wm = g & 1, wn = g >> 1, q = (lane2 & 15) >> 2, p = lane2 & 3;
  const unsigned vkv = g * 2048 + lane2 * 16;
  const unsigned vo0 = (16 * g + (lane2 >> 4)) * 2048 + (((lane2 & 15) ^ ((lane2 >> 4) << 1)) << 4), vo1 = vo0 ^ 128;
  const unsigned vpf = ((idx * 4 + g) * 128 + lane2) * 128;
  const u32x4 spf = make_srd(br ? a.Bwo : a.Awo, 2u << 20);
  auto issue_w3 = [&](int s) {
#pragma unroll
    for (int i = 0; i < 4; ++i) dmap<0>((i & 1) ? vo1 : vo0, so, (p3ord(s) * 64 + 4 * i) * 2048, b4, w3slot(s) + i * 1024);
  };
  auto issue = [&](auto C) {
    constexpr int c = C;
    if constexpr (c >= 2000) {
      asm volatile("s_mov_b32 m0, %3\n\ts_nop 0\n\tbuffer_load_dword %0, %1, %2 offen lds" :: "v"(vpf), "s"(spf), "s"((c - 2000) * 8192), "s"(lds0 + L_SP + g * 256) : "memory");
    } else if constexpr (c >= 1000) {
      constexpr int st = (c - 1000) / 2, j = (c - 1000) & 1;
      dmap<1>(vkv, skv, st * KVS + j * 1024, b2, kvslot(st) + j * 1024);
    } else {
      constexpr int s = c / 16, i = c % 16;
      const int kk = (s & ~(ROT - 1)) | ((s + rot) & (ROT - 1));
      if constexpr (i < 4) dmap<0>(vx[i], sxa, kk * 128, b4, gslot(s) + i * 1024);
      else dmap<0>(vg[i - 4], swa, kk * 64 * 6144, b6, gslot(s) + 16384 + (i - 4) * 1024);
    }
  };
  const int c16 = lane2 & 15, g4 = lane2 >> 4;
  lds_cptr xa[2][2], wa[2][2];
#pragma unroll
  for (int i = 0; i < 2; ++i) {
    xa[0][i] = (lds_cptr)smem + (64 * wm + c16) * 128 + (((4 * i + g4) ^ (c16 >> 1)) << 4); xa[1][i] = opaque(xa[0][i] + 2 * GS);
    wa[0][i] = (lds_cptr)smem + 16384 + (8 * g4 + q) * 384 + (((2 * wn + i) ^ ((q >> 1) | ((g4 & 1) << 1))) << 5) + p * 8; wa[1][i] = opaque(wa[0][i] + 2 * GS);
  }
  f32x4 acc[4][6];
#pragma unroll
  for (int t = 0; t < 4; ++t)
#pragma unroll
    for (int f = 0; f < 6; ++f) acc[t][f] = f < 4 ? (f32x4){1.f, 1.f, 1.f, 1.f} : (f32x4){};
  const int head = 2 * hp + wn;
  const auto rkv = __builtin_amdgcn_make_buffer_rsrc(ws + WS_KVP, 0, 2u << 20, 0x00020000);
  const auto rz = __builtin_amdgcn_make_buffer_rsrc(ws + WS_ZP, 0, 65536, 0x00020000);
  const auto roa = __builtin_amdgcn_make_buffer_rsrc(ws + WS_OAF, 0, 4u << 20, 0x00020000);
  const lds_ptr L0 = (lds_ptr)smem, fl = L0 + L_FLG;
  bf16x8 xf[2][4][2]; s16x4 wf[2][4];
  auto rd1 = [&](int u, int part) {
    const int s = u / 6, f = u % 6, sl = gslot(s) / GS, hi = sl >> 1, off = (sl & 1) * GS, nx = f < 2 ? 4 : 0;
    const int xi = f == 0 ? part - 4 : part, wi = f == 0 ? part : part - nx;
    if (f < 2 && xi >= 0 && xi < 4) xf[s & 1][2 * f + (xi >> 1)][xi & 1] = ldsr128(xa[hi][xi & 1] + off + (2 * f + (xi >> 1)) * 2048);
    else if (wi >= 0 && wi < 4) wf[u & 1][wi] = ldstr(wa[hi][f & 1] + off + (f >> 1) * 128 + (wi >> 1) * 12288 + (wi & 1) * 1536);
  };
  auto mm1 = [&](int u, int m) {
    const int f = u % 6, t = m >> 1, ks = m & 1; const bf16x8 wv = cat8(wf[u & 1][2 * ks], wf[u & 1][2 * ks + 1]), xv = xf[(u / 6) & 1][t][ks];
    if (f < 2) acc[t][f] = MFMA16(wv, xv, acc[t][f]);
    else acc[t][f] = MFMA16(xv, wv, acc[t][f]);
  };
  f32x4 kv[4] = {}, z[2] = {};
  auto state_part = [&](int tp) {
    const bf16x8 ones = {0x3f80, 0x3f80, 0x3f80, 0x3f80, 0x3f80, 0x3f80, 0x3f80, 0x3f80};
    const bf16x8 v0 = pack44(acc[2 * tp][4], acc[2 * tp + 1][4]), v1 = pack44(acc[2 * tp][5], acc[2 * tp + 1][5]);
#pragma unroll
    for (int d = 0; d < 2; ++d) { const bf16x8 pk = pack44(phi4(acc[2 * tp][2 + d]), phi4(acc[2 * tp + 1][2 + d]));
      kv[d] = MFMA16(pk, v0, kv[d]); kv[2 + d] = MFMA16(pk, v1, kv[2 + d]); z[d] = MFMA16(pk, ones, z[d]); PIN(kv[d]); PIN(kv[2 + d]); PIN(z[d]); }
  };
  const lds_ptr X = L0 + 32768 + wn * 4096, XZ = L0 + L_SP + 1024 + wn * 128;
  wait_bar<20>();
  static_for<NINT>([&](auto J) {
    constexpr int j = J, n = j - NPRE, nu = units_in(j), u0 = SCH.u0[j], nop = SCH.nop[j];
    if constexpr (nu == 0) static_for<nop>([&](auto I) { issue(std::integral_constant<int, SCH.op[j][I]>{}); });
    else {
      constexpr int per = (nop + 4 * nu - 1) / (4 * nu);
      static_for<nu>([&](auto KK) {
        constexpr int kk = KK, u = u0 + kk;
        static_for<8>([&](auto M) {
          constexpr int m = M;
          if constexpr (u > 0) mm1(u - 1, m);
          SBAR();
          if constexpr (kk + 1 < nu || u % 6 >= 2) rd1(u, m); else if constexpr (m < 4) { rd1(u, 2 * m); rd1(u, 2 * m + 1); }
          if constexpr (m & 1) static_for<per>([&](auto Q) { constexpr int o = (4 * kk + (m >> 1)) * per + Q; if constexpr (o < nop) issue(std::integral_constant<int, SCH.op[j][o]>{}); });
          SBAR();
        });
      });
    }
    if constexpr (n == GEND) {
#pragma unroll
      for (int m = 0; m < 8; ++m) mm1(95, m);
    }
    if constexpr (n == NIV - 2) { __builtin_amdgcn_s_setprio(3); state_part(0); }
    if constexpr (n == NIV - 1) {
      state_part(1);
      if (wm) {
#pragma unroll
        for (int e = 0; e < 4; ++e) *(LAS f32x4*)(X + e * 1024 + lane * 16) = kv[e];
        if (c16 == 0) { *(LAS f32x4*)(XZ + g4 * 32) = z[0]; *(LAS f32x4*)(XZ + g4 * 32 + 16) = z[1]; }
      }
    }
    wait_bar<SCH.wait[j]>();
    if constexpr (j == NPRE - 1) __builtin_amdgcn_s_setprio(0);
  });
  const u64 tagv = *(const volatile LAS u64*)(fl + FL_TAG);
  const u64 tagu = ((u64)(unsigned)__builtin_amdgcn_readfirstlane((unsigned)(tagv >> 32)) << 32) | (unsigned)__builtin_amdgcn_readfirstlane((unsigned)tagv);
  if (!wm) {
#pragma unroll
    for (int e = 0; e < 4; ++e)
      __builtin_amdgcn_raw_buffer_store_b128(__builtin_bit_cast(u32x4, fadd4(kv[e], *(const LAS f32x4*)(X + e * 1024 + lane * 16))), rkv, (head * 16 + rt) * 4096 + e * 1024 + lane * 16, 0, 16);
    const bool z1 = c16 & 1;
    const f32x4 zz = fadd4((f32x4){z1 ? z[1][0] : z[0][0], z1 ? z[1][1] : z[0][1], z1 ? z[1][2] : z[0][2], z1 ? z[1][3] : z[0][3]}, *(const LAS f32x4*)(XZ + g4 * 32 + (c16 & 1) * 16));
    __builtin_amdgcn_raw_buffer_store_b128(__builtin_bit_cast(u32x4, zz), rz, c16 < 2 ? (head * 16 + rt) * 128 + g4 * 32 + c16 * 16 : 0x40000000, 0, 16);
  }
  bf16x8 pq[4];
#pragma unroll
  for (int t = 0; t < 4; ++t) { pq[t] = pack44(phi4(acc[t][0]), phi4(acc[t][1])); PIN(pq[t]); }
  if (!wm) { wait_vm<0>(); if (lds_arrive(fl + FL_KVP, lane) == 1u) set_tag(ws + WS_FLG + F_KVP + (hp * 16 + rt) * 128, tagu, lane); }
  const auto rkr = __builtin_amdgcn_make_buffer_rsrc(ws + WS_KVR, 0, 65536, 0x00020000);
  const lds_ptr YZ = L0 + L_SP + 2048 + wn * 128;
  if (g == 3) { poll16((const gu64*)(ws + WS_FLG + F_KVP) + hp * 256, tagu, lane); *(volatile LAS unsigned*)(fl + FL_KVPOK) = 1u; } else lds_wait_ge(fl + FL_KVPOK, 1u);
  if (!wm) {
    u32x4 t[4];
#pragma unroll
    for (int i = 0; i < 4; ++i) t[i] = __builtin_amdgcn_raw_buffer_load_b128(rkv, (head * 16 + g4) * 4096 + rt * 256 + (lane & 15) * 16, i * 16384, 16);
    float sm[4];
#pragma unroll
    for (int e = 0; e < 4; ++e) { float v = faddn(fadd1(__uint_as_float(t[0][e]), __uint_as_float(t[1][e])), fadd1(__uint_as_float(t[2][e]), __uint_as_float(t[3][e])));
      const auto r16 = __builtin_amdgcn_permlane16_swap(__float_as_uint(v), __float_as_uint(v), false, false);
      v = faddn(__uint_as_float(r16[0]), __uint_as_float(r16[1])); sm[e] = fadd1(v, other_half(v)); }
    const u32x2 pk = {cvtpk(sm[0], sm[1]), cvtpk(sm[2], sm[3])};
    __builtin_amdgcn_raw_buffer_store_b64(pk, rkr, lane < 16 ? head * 2048 + (rt >> 3) * 1024 + ((rt & 3) * 16 + lane) * 16 + ((rt >> 2) & 1) * 8 : 0x40000000, 0, 16);
    wait_vm<0>();
    if (lds_arrive(fl + FL_KVR, lane) == 1u) set_tag(ws + WS_FLG + F_KVR + (hp * 16 + rt) * 128, tagu, lane);
  } else {
    const f32x4 z0 = __builtin_bit_cast(f32x4, __builtin_amdgcn_raw_buffer_load_b128(rz, (head * 16 + (lane & 7)) * 128 + (lane >> 3) * 16, 0, 16));
    const f32x4 z1 = __builtin_bit_cast(f32x4, __builtin_amdgcn_raw_buffer_load_b128(rz, (head * 16 + (lane & 7)) * 128 + (lane >> 3) * 16, 1024, 16));
    f32x4 zq;
#pragma unroll
    for (int e = 0; e < 4; ++e) { float v = faddn(z0[e], z1[e]); v = faddn(v, dppf<0xB1>(v)); v = faddn(v, dppf<0x4E>(v)); zq[e] = fadd1(v, dppf<0x141>(v)); }
    if ((lane & 7) == 0) *(LAS u32x2*)(YZ + (lane >> 3) * 8) = (u32x2){cvtpk(zq[0], zq[1]), cvtpk(zq[2], zq[3])};
    lds_arrive(fl + FL_Z, lane);
  }
  if (g == 3) { poll16((const gu64*)(ws + WS_FLG + F_KVR) + hp * 256, tagu, lane); *(volatile LAS unsigned*)(fl + FL_KVROK) = 1u; } else lds_wait_ge(fl + FL_KVROK, 1u);
  { const bf16x8 kf0 = __builtin_bit_cast(bf16x8, __builtin_amdgcn_raw_buffer_load_b128(rkr, head * 2048 + lane * 16, 0, 16));
    const bf16x8 kf1 = __builtin_bit_cast(bf16x8, __builtin_amdgcn_raw_buffer_load_b128(rkr, head * 2048 + lane * 16, 1024, 16));
    lds_wait_ge(fl + FL_Z, 2u);
    const bf16x8 zf = ldsr128(YZ + g4 * 16);
#pragma unroll
    for (int t = 0; t < 4; ++t) {
      const f32x4 zero = {};
      f32x4 n0 = MFMA16(kf0, pq[t], zero), n1 = MFMA16(kf1, pq[t], zero); const f32x4 den = MFMA16(zf, pq[t], zero);
      const float rcp = __builtin_amdgcn_rcpf(den[0] + 1e-6f);
#pragma unroll
      for (int i = 0; i < 4; ++i) { n0[i] = sc(n0[i] * rcp); n1[i] = sc(n1[i] * rcp); }
      st16(roa, ((4 * rt + 2 * wm + (t >> 1)) * 32 + head) * 2048 + (t & 1) * 1024 + lane * 16, pack44(n0, n1));
    } }
  __builtin_amdgcn_s_setprio(2);
  wait_vm<0>();
  if (lds_arrive(fl + FL_OA, lane) == 3u) set_tag(ws + WS_FLG + F_O + (rt * 16 + hp) * 128, tagu, lane);
  issue_w3(0); issue_w3(1);
  const u32x4 sx3 = make_srd(ws + (br ? WS_OBF : WS_OAF) + (size_t)rt * 262144, 262144);
  const unsigned v3 = g * 65536 + lane * 16;
  auto issue_x3 = [&](int s) {
#pragma unroll
    for (int i = 0; i < 4; ++i) dmap<1>(v3, sx3, p3ord(s) * 4096 + i * 1024, b4, x3slot(s) + i * 1024);
  };
  auto att_wait = [&](unsigned n) {
#pragma unroll
    for (int i = 0; i < 4; ++i) lds_wait_ge(fl + FL_TA + i * 4, n); };
  att_wait(2u);
  issue_w3(2); issue_w3(3);
  const u32x4 sfo = make_srd(ws + WS_FLG + F_O + (br * 16 + rt) * 2048, 2048);
  u32x2 ft; unsigned got = 0;
  auto poll_issue = [&]() { asm volatile("buffer_load_dwordx2 %0, %1, %2, 0 offen sc1" : "=v"(ft) : "v"((lane & 15) * 128), "s"(sfo) : "memory"); };
  auto poll_check = [&]() { got = (unsigned)__ballot(ft[0] == (unsigned)tagu && ft[1] == (unsigned)(tagu >> 32)); };
  auto poll_block = [&](unsigned m) {
#pragma unroll 1
    for (int i = 0; i < (1 << 20) && (got & m) != m; ++i) { poll_issue(); asm volatile("s_waitcnt vmcnt(0)" : "+v"(ft) :: "memory"); poll_check(); if ((got & m) != m) __builtin_amdgcn_s_sleep(4); } };
  if (g == 3) { poll_block(p3mask(0)); *(volatile LAS unsigned*)(fl + FL_OOK) = 1u; } else lds_wait_ge(fl + FL_OOK, 1u);
  issue_x3(0); issue_x3(1);
  att_wait(3u);
  issue_x3(2); issue_x3(3);
  wait_bar<12>();
  if (g == 3 && (got & p3mask(1)) != p3mask(1)) poll_issue();
  static_for<15>([&](auto S) {
    constexpr int s = S, last = s + 3 < 15 ? s + 3 : 15;
    if constexpr (s == 0 || s == 4 || s == 8) { if (g == 3) {
        if ((got & p3mask(s / 4 + 1)) != p3mask(s / 4 + 1)) { asm volatile("s_waitcnt vmcnt(%1)" : "+v"(ft) : "i"(s ? 8 : 0) : "memory"); poll_check(); poll_block(p3mask(s / 4 + 1)); }
        if (s == 8 && blockIdx.x == 1) set_tag(ws + WS_FLG + F_EPOCH, tagu, lane);
      } }
    wait_bar<(s == 0 ? 8 : (s == 1 ? 12 : 8 * (last - (s + 1))))>();
    if constexpr (s == 3 || s == 7) { if (g == 3 && (got & p3mask(s / 4 + 2)) != p3mask(s / 4 + 2)) poll_issue(); }
    if constexpr (s + 4 < 16) { issue_w3(s + 4); issue_x3(s + 4); }
  });
}

template <bool PROD>
DI void p1_compute(char* smem, const int w, const int lane, const int hp, const unsigned tag, f32x4 (&acc)[4][2], f32x4 (&acce)[4]) {
  const int wm = w & 1, wn = w >> 1, c = lane & 15, g4 = lane >> 4, q = c >> 2, p = lane & 3; const bool isv = kv_block(hp) & 1;
  lds_cptr xa[2][2], wa[2][2], xe[2], we[2][2];
#pragma unroll
  for (int i = 0; i < 2; ++i) {
    xa[0][i] = opaque((lds_cptr)smem + L_P1 + (64 * wm + c) * 128 + (((4 * i + g4) ^ (c >> 1)) << 4)); xa[1][i] = opaque(xa[0][i] + 2 * P1S);
    wa[0][i] = opaque((lds_cptr)smem + L_P1 + 16384 + (8 * g4 + q) * 128 + (((2 * wn + i) ^ ((q >> 1) | ((g4 & 1) << 1))) << 5) + p * 8); wa[1][i] = opaque(wa[0][i] + 2 * P1S);
    if (PROD) { we[0][i] = opaque((lds_cptr)smem + L_P1 + 24576 + (32 * w + 8 * g4 + q) * 64 + ((i ^ (g4 & 1)) << 5) + p * 8); we[1][i] = opaque(we[0][i] + 2 * P1S); }
  }
  if (PROD) { xe[0] = opaque((lds_cptr)smem + L_P1 + (32 * (kv_block(hp) >> 1) + c) * 128 + (((4 * w + g4) ^ (c >> 1)) << 4)); xe[1] = opaque(xe[0] + 2 * P1S); }
  bf16x8 xf[2][4][2], ef[2][2]; s16x4 wf[2][2][4], eg[2][4];
  auto rd = [&](int s, int i) {
    const int b = s & 1, hi = (s >> 1) & 1, off = (s & 1) * P1S;
    if (i < 8) wf[b][i >> 2][i & 3] = ldstr(wa[hi][i >> 2] + off + ((i >> 1) & 1) * 4096 + (i & 1) * 512);
    else if (i < 16) xf[b][(i - 8) >> 1][i & 1] = ldsr128(xa[hi][i & 1] + off + ((i - 8) >> 1) * 2048);
    else if (PROD && i < 18) ef[b][i & 1] = ldsr128(xe[hi] + off + (i & 1) * 2048);
    else if (PROD && i < 22) eg[b][i - 18] = ldstr(we[hi][(i - 18) >> 1] + off + (i & 1) * 256);
  };
  constexpr int ord[22] = {8, 0, 1, 9, 2, 3, 4, 5, 6, 7, 10, 11, 12, 13, 14, 15, 16, 17, 18, 19, 20, 21};
  bar();
#pragma unroll
  for (int i = 0; i < 22; ++i) rd(0, ord[i]);
  static_for<16>([&](auto S) { constexpr int s = S, b = s & 1;
    if constexpr (s < 15) bar();
    static_for<16>([&](auto M) { constexpr int m = M, t = m >> 2, f = (m >> 1) & 1, ks = m & 1;
      acc[t][f] = MFMA16(cat8(wf[b][f][2 * ks], wf[b][f][2 * ks + 1]), xf[b][t][ks], acc[t][f]); SBAR();
      if constexpr (s < 15 && m < 11) { rd(s + 1, ord[2 * m]); rd(s + 1, ord[2 * m + 1]); }
      SBAR(); });
    if (PROD) {
      const bf16x8 w0 = cat8(eg[b][0], eg[b][1]), w1 = cat8(eg[b][2], eg[b][3]);
      const bf16x8 a0 = isv ? ef[b][0] : w0, a1 = isv ? ef[b][1] : w1, b0 = isv ? w0 : ef[b][0], b1 = isv ? w1 : ef[b][1];
      acce[0] = MFMA16(a0, b0, acce[0]); acce[1] = MFMA16(a0, b1, acce[1]); acce[2] = MFMA16(a1, b0, acce[2]); acce[3] = MFMA16(a1, b1, acce[3]); SBAR();
    } });
  if (PROD && w) {
#pragma unroll
    for (int e = 0; e < 4; ++e) *(LAS f32x4*)((lds_ptr)smem + L_KVX + e * 1024 + lane * 16) = acce[e];
    asm volatile("s_waitcnt lgkmcnt(0)" ::: "memory");
    *(volatile LAS unsigned*)((lds_ptr)smem + L_P1F) = tag;
  }
}
template <bool SW16> DI bf16x8 pair16(const f32x4 x0, const f32x4 x1) {
  const unsigned a0 = cvtpk(x0[0], x0[1]), a1 = cvtpk(x0[2], x0[3]), b0 = cvtpk(x1[0], x1[1]), b1 = cvtpk(x1[2], x1[3]);
  if constexpr (SW16) { const auto r0 = __builtin_amdgcn_permlane16_swap(a0, b0, false, false), r1 = __builtin_amdgcn_permlane16_swap(a1, b1, false, false);
    return __builtin_bit_cast(bf16x8, (u32x4){r0[0], r1[0], r0[1], r1[1]}); }
  else { const auto r0 = __builtin_amdgcn_permlane32_swap(a0, b0, false, false), r1 = __builtin_amdgcn_permlane32_swap(a1, b1, false, false);
    return __builtin_bit_cast(bf16x8, (u32x4){r0[0], r1[0], r0[1], r1[1]}); }
}
#define EX(x) __builtin_amdgcn_exp2f(x)
DI void compute_role(char* smem, const Args& a, const int w, const int lane) {
  char* ws = a.ws;
  const lds_ptr fl = (lds_ptr)smem + L_FLG;
  u64 tag; asm volatile("s_load_dwordx2 %0, %1, 0x0\n\ts_waitcnt lgkmcnt(0)" : "=s"(tag) : "s"(ws + WS_FLG + F_EPOCH) : "memory");
  tag += 1u;
  if (w == 1) *(volatile LAS unsigned*)((lds_ptr)smem + L_P1F) = ~(unsigned)tag;
  const int xcd = blockIdx.x & 7, idx = blockIdx.x >> 3;
  const int rt = (xcd >> 1) * 4 + (idx & 3), hp = (xcd & 1) * 8 + (idx >> 2);
  const int wm = w & 1, wn = w >> 1;
  const unsigned lds0 = (unsigned)(uintptr_t)smem;
  bf16x8 Qa0, Qa1, Qb0, Qb1; f32x4 acce[4] = {}, acc[4][2] = {};
  if (kv_block(hp) < 0 || w >= 2) p1_compute<false>(smem, w, lane, hp, (unsigned)tag, acc, acce); else p1_compute<true>(smem, w, lane, hp, (unsigned)tag, acc, acce);
  const bool prod = kv_block(hp) >= 0 && w == 0;
  const int kvid = 8 * rt + kv_block(hp);
  bf16x8 f0, f1;
  if (prod) {
#pragma unroll 1
    for (int i = 0; i < (1 << 22); ++i) { if ((unsigned)__builtin_amdgcn_readfirstlane(*(const volatile LAS unsigned*)((lds_cptr)smem + L_P1F)) == (unsigned)tag) break; }
    asm volatile("" ::: "memory");
#pragma unroll
    for (int e = 0; e < 4; ++e) acce[e] += *(const LAS f32x4*)((lds_cptr)smem + L_KVX + e * 1024 + lane * 16);
    unsigned lo = lane * 16;
    if (kvid & 1) { f0 = pair16<false>(acce[0], acce[1]); f1 = pair16<false>(acce[2], acce[3]); lo = ((lane >> 5) * 16 + (lane & 15) + ((lane >> 4) & 1) * 32) * 16; }
    else { f0 = pair16<true>(acce[0], acce[1]); f1 = pair16<true>(acce[2], acce[3]); }
    const auto rk = __builtin_amdgcn_make_buffer_rsrc(ws + WS_KVF, 0, 262144, 0x00020000);
    st16(rk, kvid * 2048 + lo, f0); st16(rk, kvid * 2048 + 1024 + lo, f1);
  }
#pragma unroll
  for (int t = 0; t < 4; ++t) { acc[t][0] *= QSCALE; acc[t][1] *= QSCALE; }
  Qa0 = pair16<true>(acc[0][0], acc[1][0]); Qa1 = pair16<true>(acc[0][1], acc[1][1]); Qb0 = pair16<true>(acc[2][0], acc[3][0]); Qb1 = pair16<true>(acc[2][1], acc[3][1]);
  float na = 0.f, nb = 0.f;
  { const u32x4 a0 = __builtin_bit_cast(u32x4, Qa0), a1 = __builtin_bit_cast(u32x4, Qa1), b0 = __builtin_bit_cast(u32x4, Qb0), b1 = __builtin_bit_cast(u32x4, Qb1);
#pragma unroll
    for (int j = 0; j < 4; ++j) {
      na += bflo(a0[j]) * bflo(a0[j]) + bfhi(a0[j]) * bfhi(a0[j]) + bflo(a1[j]) * bflo(a1[j]) + bfhi(a1[j]) * bfhi(a1[j]);
      nb += bflo(b0[j]) * bflo(b0[j]) + bfhi(b0[j]) * bfhi(b0[j]) + bflo(b1[j]) * bflo(b1[j]) + bfhi(b1[j]) * bfhi(b1[j]);
    } }
  na += other_half(na); nb += other_half(nb); PIN(na); PIN(nb);
  unsigned vx[4], vg[6], b4 = lds0 + w * 4096, b6 = lds0 + w * 6144;
  asm volatile("" : "+s"(b4), "+s"(b6));
  x_offsets(vx, w, lane); g_offsets(vg, w, lane);
  const u32x4 sxa = make_srd(a.Ax + (size_t)rt * 128 * 1024, 128 * 2048), swa = make_srd(a.Awqkv + hp * 64, 1024 * 6144 - hp * 128);
  const u32x4 sfl = make_srd(ws + WS_FLG + F_KV, 16384);
  if (w == 2) wait_vm<0>();
  bar();
  if (prod) {
    float mx;
    const u32x4 u0 = __builtin_bit_cast(u32x4, f0), u1 = __builtin_bit_cast(u32x4, f1);
    float n2 = 0.f, am = 0.f;
#pragma unroll
    for (int j = 0; j < 4; ++j) {
      const float c0 = bflo(u0[j]), c1 = bfhi(u0[j]), c2 = bflo(u1[j]), c3 = bfhi(u1[j]);
      n2 += c0 * c0 + c1 * c1 + c2 * c2 + c3 * c3;
      am = fmaxf(fmaxf(am, fmaxf(fabsf(c0), fabsf(c1))), fmaxf(fabsf(c2), fabsf(c3)));
    }
    n2 += other_half(n2);
    mx = (kvid & 1) ? am : n2;
    if (!(n2 == n2)) mx = INFINITY;
    mx = wave_max(mx);
    wait_vm<0>();
    const u32x4 gr = {(unsigned)tag, (unsigned)(tag >> 32), __float_as_uint(mx), 0u};
    __builtin_amdgcn_raw_buffer_store_b128(gr, __builtin_amdgcn_make_buffer_rsrc(ws + WS_FLG + F_KV, 0, 16384, 0x00020000), lane == 0 ? kvid * 128 : 0x40000000, 0, 16);
  }
  u32x4 g0, g1;
  auto poll_issue = [&]() { asm volatile("buffer_load_dwordx4 %0, %2, %3, 0 offen sc1\n\tbuffer_load_dwordx4 %1, %2, %3, 0 offen offset:128 sc1" : "=&v"(g0), "=&v"(g1) : "v"(lane * 256), "s"(sfl) : "memory"); };
  auto poll_check = [&]() {
    const bool hit = __all(g0[0] == (unsigned)tag && g0[1] == (unsigned)(tag >> 32) && g1[0] == (unsigned)tag && g1[1] == (unsigned)(tag >> 32));
    if (hit) { const float km = wave_max(__uint_as_float(g0[2])), vm = wave_max(__uint_as_float(g1[2]));
      *(volatile LAS float*)(fl + FL_KMAX) = km; *(volatile LAS float*)(fl + FL_KMAX + 4) = vm;
      asm volatile("s_waitcnt lgkmcnt(0)" ::: "memory");
      *(volatile LAS unsigned*)(fl + FL_KVOK) = 1u; }
    return hit; };
  bool seen = false;
  static_for<NPRE - 1>([&](auto J) { constexpr int j = J;
    if constexpr (j == NPRE - 2) { if (w == 3) { poll_issue();
      *(volatile LAS unsigned*)(fl + lane * 4) = lane == FL_TAG / 4 ? (unsigned)tag : (lane == FL_TAG / 4 + 1 ? (unsigned)(tag >> 32) : 0u); } }
    if constexpr (SCH.cst[j] >= 0) issue_g(SCH.cst[j], (idx & 3) + (idx >> 2), vx, vg, sxa, swa, b4, b6);
    if constexpr (j == NPRE - 2) { if (w == 3) { asm volatile("s_waitcnt vmcnt(%2)" : "+v"(g0), "+v"(g1) : "i"(SCH.cst[j] >= 0 ? 10 : 0) : "memory"); seen = poll_check(); } }
    wait_bar<SCH.cwait[j]>();
  });
  if (w == 3) {
#pragma unroll 1
    for (int i = 0; i < (1 << 20) && !seen; ++i) { poll_issue(); asm volatile("s_waitcnt vmcnt(0)" : "+v"(g0), "+v"(g1) :: "memory"); seen = poll_check(); }
  }
  lds_wait_ge(fl + FL_KVOK, 1u);
  const float kmax = *(volatile LAS float*)(fl + FL_KMAX), vmax = *(volatile LAS float*)(fl + FL_KMAX + 4);
  { const u32x4 skv = make_srd(ws + WS_KVF, 262144);
#pragma unroll
    for (int i = 0; i < 8; ++i) dmap<1>(w * 2048 + lane * 16, skv, (i >> 1) * KVS + (i & 1) * 1024, lds0 + w * 2048, L_RING + (i >> 1) * KVS + (i & 1) * 1024); }
  const bool ok = (na * kmax <= 3500.f) && (nb * kmax <= 3500.f) && (vmax >= 0x1p-40f) && (vmax <= 0x1p50f);
  const bool fast = __all(ok);
  f32x16 Oa = {}, Ob = {}; f32x4 La = {}, Lb = {};
  if (fast) wait_bar<6>(); else wait_bar<0>();
  const unsigned fa = lds0 + L_FLG + FL_TA + w * 4;
  if (fast) {
    const short lo = ((lane >> 2) & 1) ? (short)0 : (short)0x3f80, hi = ((lane >> 2) & 1) ? (short)0x3f80 : (short)0;
    const bf16x8 sel = {lo, lo, lo, lo, hi, hi, hi, hi};
    const unsigned sidx = (lane & 1) ? 0xEEEEEEEEu : 0x44444444u;
    unsigned vc = lds0 + L_RING + lane * 16, vn = vc, ta = lds0 + L_TA + lane * 16, tb = lds0 + L_TB + lane * 16, tc = lds0 + L_TC + lane * 16, cnt, fv = 0;
    asm volatile(ATTN_ASM : "+v"(Oa), "+v"(Ob), "+v"(La), "+v"(Lb), "+v"(vc), "+v"(vn), "=&s"(cnt), "+v"(fv)
                 : "v"(Qa0), "v"(Qa1), "v"(Qb0), "v"(Qb1), "v"(sel), "v"(ta), "v"(tb), "v"(fa), "v"(tc), "v"(sidx) : ATTN_CLOB);
    La[0] += La[1]; Lb[0] += Lb[1];
  } else {
    const short one = (((lane >> 4) ^ (lane >> 2)) & 1) ? (short)0 : (short)0x3f80;
    const bf16x8 sel = {one, one, one, one, one, one, one, one};
    const f32x16 zero16 = {};
    float ma = -INFINITY, mb = -INFINITY;
#pragma unroll 1
    for (int n = 0; n < 32; ++n) {
      const lds_cptr c = (lds_cptr)smem + lane * 16 + kvslot(n);
#pragma unroll
      for (int kt = 0; kt < 2; ++kt) {
        const bf16x8 K0 = ldsr128(c + kt * 4096), K1 = ldsr128(c + kt * 4096 + 1024), V0 = ldsr128(c + kt * 4096 + 2048), V1 = ldsr128(c + kt * 4096 + 3072);
#pragma unroll
        for (int t = 0; t < 2; ++t) {
          f32x16 S = MFMA32(K0, t ? Qb0 : Qa0, zero16); S = MFMA32(K1, t ? Qb1 : Qa1, S);
          float m1 = S[0];
#pragma unroll
          for (int i = 1; i < 16; ++i) m1 = fmaxf(m1, S[i]);
          m1 = fmaxf(m1, __shfl_xor(m1, 32));
          float& m = t ? mb : ma; f32x16& O = t ? Ob : Oa; f32x4& L = t ? Lb : La;
          const float mn = fmaxf(m, m1), al = EX(m - mn); m = mn;
#pragma unroll
          for (int i = 0; i < 16; ++i) { S[i] = EX(S[i] - mn); O[i] *= al; }
          L *= al;
          const bf16x8 P0 = pack8(S, 0), P1 = pack8(S, 1);
          O = MFMA32(V0, P0, O); O = MFMA32(V1, P1, O); L = MFMA16(sel, P0, L); L = MFMA16(sel, P1, L);
        }
      }
      if (n < NIV) bar();
    }
    asm volatile("s_waitcnt lgkmcnt(0)" ::: "memory");
    *(volatile LAS unsigned*)(uintptr_t)fa = 3u;
  }
  const auto rob = __builtin_amdgcn_make_buffer_rsrc(ws + WS_OBF, 0, 4u << 20, 0x00020000);
  { const float ra = __builtin_amdgcn_rcpf(La[0]), rb = __builtin_amdgcn_rcpf(Lb[0]);
#pragma unroll
    for (int i = 0; i < 16; ++i) { Oa[i] *= ra; Ob[i] *= rb; }
    const unsigned off = ((4 * rt + 2 * wm) * 32 + 2 * hp + wn) * 2048 + o_lane(lane);
    st16(rob, off, pack8(Oa, 0)); st16(rob, off + 512, pack8(Oa, 1)); st16(rob, off + 65536, pack8(Ob, 0)); st16(rob, off + 65536 + 512, pack8(Ob, 1));
  }
  const int br = (xcd & 1) ^ 1;
  wait_vm<0>();
  if (lds_arrive(fl + FL_OB, lane) == 3u) set_tag(ws + WS_FLG + F_O + ((16 + rt) * 16 + hp) * 128, tag, lane);
  {
    const int ct = idx >> 2, c = lane & 15, g4 = lane >> 4, q = c >> 2, p = lane & 3, kq = 4 * (g4 & 1) + q;
    const lds_cptr xa0 = opaque((lds_cptr)smem + L_TA + wm * 8192 + lane * 16), xa1 = opaque(xa0 + (L_TC - L_TA));
    lds_cptr wa[2][4], wb[2][4];
#pragma unroll
    for (int f = 0; f < 4; ++f) { wa[0][f] = opaque((lds_cptr)smem + L_TB + ((br ? 16 : 8) * (g4 >> 1) + kq) * 256 + (((2 * ((4 * wn + f) ^ kq)) + (p >> 1)) << 4) + 8 * (p & 1)); wa[1][f] = opaque(wa[0][f] + (L_RING - L_TB));
      wb[0][f] = opaque(wa[0][f] + (br ? 2048 : 4096)); wb[1][f] = opaque(wa[1][f] + (br ? 2048 : 4096)); }
    f32x4 acc[4][4] = {}; bf16x8 xf[2][2][4]; s16x4 wl[2][2][4], wh[2][2][4];
    auto rd = [&](int s, int ks, int i) {
      const int b = s & 1, hi = (s & 3) < 2, so = (s & 1) * 16384;
      if (i < 4) xf[b][ks][i] = ldsr128((hi ? xa0 : xa1) + so + (i >> 1) * 4096 + ks * 2048 + (i & 1) * 1024);
      else if (i & 1) wh[b][ks][(i - 4) >> 1] = ldstr(wb[hi][(i - 4) >> 1] + so + ks * 8192);
      else wl[b][ks][(i - 4) >> 1] = ldstr(wa[hi][(i - 4) >> 1] + so + ks * 8192);
    };
    constexpr int ord[12] = {0, 4, 5, 6, 7, 8, 9, 10, 11, 1, 2, 3};
    bar();
#pragma unroll
    for (int i = 0; i < 24; ++i) rd(0, i & 1, ord[i >> 1]);
    const lds_ptr st = (lds_ptr)smem + L_TA + w * 8192;
    const auto ro = __builtin_amdgcn_make_buffer_rsrc(a.out, 0, 8u << 20, 0x00020000);
    const unsigned ooff = ((rt * 128 + 64 * wm + (lane >> 3)) * 2048 + br * 1024 + ct * 128 + 64 * wn) * 2 + (lane & 7) * 16;
    u32x4 ov[2];
    auto epi = [&](int t, int ph) {
      if (ph < 4) { const u32x2 v = {cvtpk(acc[t][ph][0], acc[t][ph][1]), cvtpk(acc[t][ph][2], acc[t][ph][3])};
        *(LAS u32x2*)(st + (16 * t + c) * 128 + (((2 * ph + (g4 >> 1)) ^ (c & 7)) << 4) + 8 * (g4 & 1)) = v; }
      else if (ph < 6) ov[ph - 4] = *(const LAS u32x4*)(st + (16 * t + 8 * (ph - 4) + (lane >> 3)) * 128 + (((lane & 7) ^ ((lane >> 3) & 7)) << 4));
      else __builtin_amdgcn_raw_buffer_store_b128(ov[ph - 6], ro, ooff, (16 * t + 8 * (ph - 6)) * 4096, 18);
    };
    static_for<16>([&](auto S) { constexpr int s = S, b = s & 1;
      if constexpr (s < 15) bar();
      static_for<32>([&](auto M) { constexpr int m = M, t = m >> 3, f = (m >> 1) & 3, ks = m & 1;
        acc[t][f] = MFMA16(cat8(wl[b][ks][f], wh[b][ks][f]), xf[b][ks][t], acc[t][f]); SBAR();
        if constexpr (s < 15 && m < 24) rd(s + 1, m & 1, ord[m >> 1]);
        if constexpr (s == 15 && t > 0) epi(t - 1, m & 7);
        SBAR(); }); });
#pragma unroll
    for (int ph = 0; ph < 8; ++ph) epi(3, ph);
  }
}

__global__ __launch_bounds__(512, 2) void fused_kernel(Args a) {
  extern __shared__ __attribute__((aligned(16))) char smem[];
  const int lane = threadIdx.x & 63, w = __builtin_amdgcn_readfirstlane(threadIdx.x >> 6);
  if (w == 2) {
    unsigned long long pc; asm volatile("s_getpc_b64 %0" : "=s"(pc));
    const u32x4 sc = make_srd((const void*)(pc & ~255ull), CODE_PF);
    asm volatile("s_mov_b32 m0, %2\n\ts_nop 0\n\tbuffer_load_dword %0, %1, 0 offen lds" :: "v"(((blockIdx.x >> 3) + 32 * lane) * 128), "s"(sc), "s"((unsigned)(uintptr_t)smem + L_P1F + 256) : "memory");
  }
  if (w < 4) compute_role(smem, a, w, lane);
  else loader_role(smem, a, w - 4, lane);
}

extern "C" void kernel_launch(void* const* d_in, const int* in_sizes, int n_in, void* d_out, int out_size, void* d_ws, size_t ws_size, hipStream_t stream) {
  Args a;
  a.Ax = (const u16*)d_in[0]; a.Awqkv = (const u16*)d_in[1]; a.Awo = (const u16*)d_in[2];
  a.Bx = (const u16*)d_in[3]; a.Bwq = (const u16*)d_in[4]; a.Bwk = (const u16*)d_in[5]; a.Bwv = (const u16*)d_in[6]; a.Bwo = (const u16*)d_in[7];
  a.ws = (char*)d_ws; a.out = (u16*)d_out;
  (void)hipFuncSetAttribute((const void*)fused_kernel, hipFuncAttributeMaxDynamicSharedMemorySize, LDS_BYTES);
  fused_kernel<<<256, 512, LDS_BYTES, stream>>>(a);

}
```
